```python
import torch

def _softmax(x, axis=-1):
    return torch.softmax(x, dim=axis)
def _gelu(x):
    return torch.nn.functional.gelu(x, approximate="tanh")
def _silu(x):
    return torch.nn.functional.silu(x)
def _sigmoid(x):
    return torch.sigmoid(x)
def _layernorm(x, axis=-1, eps=1e-5):
    m = x.mean(dim=axis, keepdim=True)
    v = x.var(dim=axis, keepdim=True, unbiased=False)
    return (x - m) / torch.sqrt(v + eps)
def _scatter_add(tgt, idx, src, axis=0):
    return tgt.clone().index_add_(axis, idx, src)
def _segment_sum(src, seg, n):
    out = torch.zeros((n,) + src.shape[1:], dtype=src.dtype)
    return out.index_add_(0, seg, src)
def _pad(x, pw):
    flat = tuple(v for pair in reversed(pw) for v in pair)
    return torch.nn.functional.pad(x, flat)

kernel_name = 'concat_heads__softmax_full_mha__sliding_block__prompt'

def setup_inputs():

    _w = (lambda a: (lambda t: t.to(torch.bfloat16) if t.is_floating_point() else t)(torch.as_tensor(a)))
    def _A_setup():
        B, S, D = 1, 2048, 640
        return {
            "x": _w((lambda *s: torch.randn(*s, dtype=torch.bfloat16) * 0.2)(B, S, D)),
            "wqkv": _w((lambda *s: torch.randn(*s, dtype=torch.bfloat16) * 0.4)(D, 3 * D)),
            "wo": _w((lambda *s: torch.randn(*s, dtype=torch.bfloat16) * 0.1)(D, D)),
        }
    _A_in = _A_setup()

    def _B_setup():
        B, S, D = 1, 2048, 640
        return {
            "x": _w((lambda *s: torch.randn(*s, dtype=torch.bfloat16) * 0.4)(B, S, D)),
            "wqkv": _w((lambda *s: torch.randn(*s, dtype=torch.bfloat16) * 0.4)(D, 3 * D)),
            "wo": _w((lambda *s: torch.randn(*s, dtype=torch.bfloat16) * 0.05)(D, D)),
        }
    _B_in = _B_setup()

    return {
        "A_x": _A_in["x"],
        "A_wqkv": _A_in["wqkv"],
        "A_wo": _A_in["wo"],
        "B_x": _B_in["x"],
        "B_wqkv": _B_in["wqkv"],
        "B_wo": _B_in["wo"],
    }

def reference(A_x, A_wqkv, A_wo, B_x, B_wqkv, B_wo):
    def _A_ref(x, wqkv, wo):
        B, S, D = x.shape
        H = 32; Dh = D // H
        qkv = (lambda x, *d: x.permute(*d))((x @ wqkv).reshape(B, S, 3, H, Dh), 2, 0, 3, 1, 4)
        q, k, v = qkv[0], qkv[1], qkv[2]
        scale = Dh ** -0.5
        scores = torch.einsum("bhqd,bhkd->bhqk", q, k) * scale
        p = _softmax(scores, axis=-1)
        o = torch.einsum("bhqk,bhkd->bhqd", p, v)
        return (lambda x, *d: x.permute(*d))(o, 0, 2, 1, 3).reshape(B, S, D) @ wo

    def _B_ref(x, wqkv, wo):
        B, S, D = x.shape
        H = 32; Dh = D // H; W = 256
        qkv = (lambda x, *d: x.permute(*d))((x @ wqkv).reshape(B, S, 3, H, Dh), 2, 0, 3, 1, 4)
        q, k, v = qkv[0], qkv[1], qkv[2]
        scores = torch.einsum("bhqd,bhkd->bhqk", q, k) * (Dh ** -0.5)
        idx = torch.arange(S)
        mask = (idx[:, None] - idx[None, :] >= 0) & (idx[:, None] - idx[None, :] < W)
        scores = torch.where(mask[None, None], scores, -1e9)
        p = _softmax(scores, axis=-1)
        o = torch.einsum("bhqk,bhkd->bhqd", p, v)
        return (lambda x, *d: x.permute(*d))(o, 0, 2, 1, 3).reshape(B, S, D) @ wo

    a = _A_ref(A_x, A_wqkv, A_wo)
    b = _B_ref(B_x, B_wqkv, B_wo)
    return torch.concatenate([a, b], axis=-1)
```

```cpp
#include <hip/hip_runtime.h>
#include <hip/hip_bf16.h>
typedef __hip_bfloat16 bf16;
constexpr int S = 2048, D = 640, H = 32, DH = 20, W = 256;

__global__ void gemm_naive(const bf16* A, const bf16* B, bf16* C, int M, int N, int K, int ldc) {
  __shared__ float As[16][17], Bs[16][17];
  int r = blockIdx.y * 16 + threadIdx.y, c = blockIdx.x * 16 + threadIdx.x;
  float acc = 0.f;
  for (int k0 = 0; k0 < K; k0 += 16) {
    As[threadIdx.y][threadIdx.x] = __bfloat162float(A[r * K + k0 + threadIdx.x]);
    Bs[threadIdx.y][threadIdx.x] = __bfloat162float(B[(k0 + threadIdx.y) * N + c]);
    __syncthreads();
    for (int k = 0; k < 16; k++) acc += As[threadIdx.y][k] * Bs[k][threadIdx.x];
    __syncthreads();
  }
  C[r * ldc + c] = __float2bfloat16(acc);
}
__global__ void attn_naive(const bf16* qkv, bf16* o, int windowed) {
  int q = blockIdx.x * 64 + threadIdx.x, h = blockIdx.y;
  float qv[DH], acc[DH];
  for (int d = 0; d < DH; d++) { qv[d] = __bfloat162float(qkv[q * 3 * D + h * DH + d]); acc[d] = 0.f; }
  float m = -1e30f, l = 0.f; const float scale = 0.22360679774997896f;
  int k0 = windowed ? max(0, q - W + 1) : 0, k1 = windowed ? q + 1 : S;
  for (int k = k0; k < k1; k++) {
    float s = 0.f;
    for (int d = 0; d < DH; d++) s += qv[d] * __bfloat162float(qkv[k * 3 * D + D + h * DH + d]);
    s *= scale;
    float mn = fmaxf(m, s), a = __expf(m - mn), p = __expf(s - mn);
    l = l * a + p;
    for (int d = 0; d < DH; d++) acc[d] = acc[d] * a + p * __bfloat162float(qkv[k * 3 * D + 2 * D + h * DH + d]);
    m = mn;
  }
  for (int d = 0; d < DH; d++) o[q * D + h * DH + d] = __float2bfloat16(acc[d] / l);
}

extern "C" void kernel_launch(void* const* d_in, const int* in_sizes, int n_in, void* d_out, int out_size, void* d_ws, size_t ws_size, hipStream_t stream) {
  bf16* out = (bf16*)d_out;
  bf16* qkv = (bf16*)d_ws; bf16* o = qkv + S * 3 * D;
  for (int b = 0; b < 2; b++) {
    const bf16* x = (const bf16*)d_in[3 * b]; const bf16* wqkv = (const bf16*)d_in[3 * b + 1]; const bf16* wo = (const bf16*)d_in[3 * b + 2];
    gemm_naive<<<dim3(3 * D / 16, S / 16), dim3(16, 16), 0, stream>>>(x, wqkv, qkv, S, 3 * D, D, 3 * D);
    attn_naive<<<dim3(S / 64, H), 64, 0, stream>>>(qkv, o, b);
    gemm_naive<<<dim3(D / 16, S / 16), dim3(16, 16), 0, stream>>>(o, wo, out + b * D, S, D, D, 2 * D);
  }
}
```

```cpp
#include <hip/hip_runtime.h>
#include <hip/hip_bf16.h>
#include <stdint.h>

typedef __attribute__((ext_vector_type(8))) __bf16 bf16x8;
typedef __attribute__((ext_vector_type(16))) __bf16 bf16x16;
typedef __attribute__((ext_vector_type(2))) __bf16 bf16x2;
typedef __attribute__((ext_vector_type(4))) short s16x4;
typedef __attribute__((ext_vector_type(2))) float f32x2;
typedef __attribute__((ext_vector_type(8))) float f32x8;
typedef __attribute__((ext_vector_type(16))) float f32x16;
typedef __attribute__((ext_vector_type(2))) unsigned u32x2;
typedef __attribute__((ext_vector_type(3))) unsigned u32x3;
typedef __attribute__((ext_vector_type(4))) unsigned u32x4;
typedef __attribute__((ext_vector_type(6))) unsigned u32x6;
typedef __attribute__((ext_vector_type(7))) unsigned u32x7;
typedef __attribute__((ext_vector_type(12))) unsigned u32x12;
typedef __attribute__((ext_vector_type(16))) unsigned u32x16;
typedef unsigned long long u64;
#define GLB_AS __attribute__((address_space(1)))
typedef __amdgpu_buffer_rsrc_t rsrc_t;

#define LOAD_ASM \
  "s_mov_b64 s[58:59], s[52:53]\n\t" \
  "s_and_b32 s57, s58, 15\n\t" \
  "s_lshr_b64 s[58:59], s[58:59], 4\n\t" \
  "s_mov_b32 m0, s54\n\t" \
  "s_lshl_b32 s56, s57, 7\n\t" \
  "buffer_load_dwordx4 v10, s[40:43], s56 offen offset:0 lds\n\t" \
  "buffer_load_dwordx4 v11, s[40:43], s56 offen offset:1024 lds\n\t" \
  "buffer_load_dwordx4 v12, s[40:43], s56 offen offset:2048 lds\n\t" \
  "buffer_load_dwordx4 v13, s[40:43], s56 offen offset:3072 lds\n\t" \
  "s_add_u32 m0, s54, 16384\n\t" \
  "s_mul_i32 s57, s57, 0x3c000\n\t" \
  "buffer_load_dwordx4 v14, s[44:47], s57 offen offset:0 lds\n\t" \
  "buffer_load_dwordx4 v15, s[44:47], s57 offen offset:1024 lds\n\t" \
  "buffer_load_dwordx4 v16, s[44:47], s57 offen offset:2048 lds\n\t" \
  "buffer_load_dwordx4 v17, s[44:47], s57 offen offset:3072 lds\n\t" \
  "s_add_u32 s54, s54, 32768\n\t" \
  "s_and_b32 s54, s54, 0x1ffff\n\t" \
  "s_and_b32 s57, s58, 15\n\t" \
  "s_lshr_b64 s[58:59], s[58:59], 4\n\t" \
  "s_mov_b32 m0, s54\n\t" \
  "s_lshl_b32 s56, s57, 7\n\t" \
  "buffer_load_dwordx4 v10, s[40:43], s56 offen offset:0 lds\n\t" \
  "buffer_load_dwordx4 v11, s[40:43], s56 offen offset:1024 lds\n\t" \
  "buffer_load_dwordx4 v12, s[40:43], s56 offen offset:2048 lds\n\t" \
  "buffer_load_dwordx4 v13, s[40:43], s56 offen offset:3072 lds\n\t" \
  "s_add_u32 m0, s54, 16384\n\t" \
  "s_mul_i32 s57, s57, 0x3c000\n\t" \
  "buffer_load_dwordx4 v14, s[44:47], s57 offen offset:0 lds\n\t" \
  "buffer_load_dwordx4 v15, s[44:47], s57 offen offset:1024 lds\n\t" \
  "buffer_load_dwordx4 v16, s[44:47], s57 offen offset:2048 lds\n\t" \
  "buffer_load_dwordx4 v17, s[44:47], s57 offen offset:3072 lds\n\t" \
  "s_add_u32 s54, s54, 32768\n\t" \
  "s_and_b32 s54, s54, 0x1ffff\n\t" \
  "v_mov_b32 v18, 0\n\t" \
  "global_load_dwordx2 v[8:9], v18, s[66:67] sc1\n\t" \
  "s_waitcnt vmcnt(9)\n\t" \
  "s_barrier\n\t" \
  "s_and_b32 s57, s58, 15\n\t" \
  "s_lshr_b64 s[58:59], s[58:59], 4\n\t" \
  "s_mov_b32 m0, s54\n\t" \
  "s_lshl_b32 s56, s57, 7\n\t" \
  "buffer_load_dwordx4 v10, s[40:43], s56 offen offset:0 lds\n\t" \
  "buffer_load_dwordx4 v11, s[40:43], s56 offen offset:1024 lds\n\t" \
  "buffer_load_dwordx4 v12, s[40:43], s56 offen offset:2048 lds\n\t" \
  "buffer_load_dwordx4 v13, s[40:43], s56 offen offset:3072 lds\n\t" \
  "s_add_u32 m0, s54, 16384\n\t" \
  "s_mul_i32 s57, s57, 0x3c000\n\t" \
  "buffer_load_dwordx4 v14, s[44:47], s57 offen offset:0 lds\n\t" \
  "buffer_load_dwordx4 v15, s[44:47], s57 offen offset:1024 lds\n\t" \
  "buffer_load_dwordx4 v16, s[44:47], s57 offen offset:2048 lds\n\t" \
  "buffer_load_dwordx4 v17, s[44:47], s57 offen offset:3072 lds\n\t" \
  "s_add_u32 s54, s54, 32768\n\t" \
  "s_and_b32 s54, s54, 0x1ffff\n\t" \
  "s_mov_b32 s55, 3\n\t" \
  "1:\n\t" \
  "s_cmp_eq_u32 s55, 10\n\t" \
  "s_cmov_b64 s[40:41], s[48:49]\n\t" \
  "s_cmov_b64 s[44:45], s[50:51]\n\t" \
  "s_cmov_b64 s[58:59], s[52:53]\n\t" \
  "s_and_b32 s57, s58, 15\n\t" \
  "s_lshr_b64 s[58:59], s[58:59], 4\n\t" \
  "s_mov_b32 m0, s54\n\t" \
  "s_lshl_b32 s56, s57, 7\n\t" \
  "buffer_load_dwordx4 v10, s[40:43], s56 offen offset:0 lds\n\t" \
  "buffer_load_dwordx4 v11, s[40:43], s56 offen offset:1024 lds\n\t" \
  "buffer_load_dwordx4 v12, s[40:43], s56 offen offset:2048 lds\n\t" \
  "buffer_load_dwordx4 v13, s[40:43], s56 offen offset:3072 lds\n\t" \
  "s_add_u32 m0, s54, 16384\n\t" \
  "s_mul_i32 s57, s57, 0x3c000\n\t" \
  "buffer_load_dwordx4 v14, s[44:47], s57 offen offset:0 lds\n\t" \
  "buffer_load_dwordx4 v15, s[44:47], s57 offen offset:1024 lds\n\t" \
  "buffer_load_dwordx4 v16, s[44:47], s57 offen offset:2048 lds\n\t" \
  "buffer_load_dwordx4 v17, s[44:47], s57 offen offset:3072 lds\n\t" \
  "s_add_u32 s54, s54, 32768\n\t" \
  "s_and_b32 s54, s54, 0x1ffff\n\t" \
  "s_add_u32 s55, s55, 1\n\t" \
  "s_cmp_lt_u32 s55, 20\n\t" \
  "s_waitcnt vmcnt(16)\n\t" \
  "s_barrier\n\t" \
  "s_cbranch_scc1 1b\n\t" \
  ""

#define GEMM_ASM \
  "v_mov_b32 v18, 0\n\t" \
  "global_load_dwordx2 v[8:9], v18, s[66:67] sc1\n\t" \
  "v_mov_b64 v[64:65], 0\n\t" \
  "v_mov_b64 v[66:67], 0\n\t" \
  "v_mov_b64 v[68:69], 0\n\t" \
  "v_mov_b64 v[70:71], 0\n\t" \
  "v_mov_b64 v[72:73], 0\n\t" \
  "v_mov_b64 v[74:75], 0\n\t" \
  "v_mov_b64 v[76:77], 0\n\t" \
  "v_mov_b64 v[78:79], 0\n\t" \
  "v_mov_b64 v[80:81], 0\n\t" \
  "v_mov_b64 v[82:83], 0\n\t" \
  "v_mov_b64 v[84:85], 0\n\t" \
  "v_mov_b64 v[86:87], 0\n\t" \
  "v_mov_b64 v[88:89], 0\n\t" \
  "v_mov_b64 v[90:91], 0\n\t" \
  "v_mov_b64 v[92:93], 0\n\t" \
  "v_mov_b64 v[94:95], 0\n\t" \
  "v_mov_b64 v[96:97], 0\n\t" \
  "v_mov_b64 v[98:99], 0\n\t" \
  "v_mov_b64 v[100:101], 0\n\t" \
  "v_mov_b64 v[102:103], 0\n\t" \
  "v_mov_b64 v[104:105], 0\n\t" \
  "v_mov_b64 v[106:107], 0\n\t" \
  "v_mov_b64 v[108:109], 0\n\t" \
  "v_mov_b64 v[110:111], 0\n\t" \
  "v_mov_b64 v[112:113], 0\n\t" \
  "v_mov_b64 v[114:115], 0\n\t" \
  "v_mov_b64 v[116:117], 0\n\t" \
  "v_mov_b64 v[118:119], 0\n\t" \
  "v_mov_b64 v[120:121], 0\n\t" \
  "v_mov_b64 v[122:123], 0\n\t" \
  "v_mov_b64 v[124:125], 0\n\t" \
  "v_mov_b64 v[126:127], 0\n\t" \
  "s_mov_b32 s61, 0\n\t" \
  "s_cmp_eq_u32 s64, 0\n\t" \
  "s_cbranch_scc1 6f\n\t" \
  "s_getpc_b64 s[62:63]\n\t" \
  "global_load_dword v28, v28, s[62:63]\n\t" \
  "s_barrier\n\t" \
  "s_barrier\n\t" \
  "s_barrier\n\t" \
  "s_barrier\n\t" \
  "s_barrier\n\t" \
  "s_barrier\n\t" \
  "s_barrier\n\t" \
  "6:\n\t" \
  "s_cmp_lg_u32 s65, 0\n\t" \
  "s_cbranch_scc1 7f\n\t" \
  "s_barrier\n\t" \
  "ds_read_b128 v[128:131], v20 offset:0\n\t" \
  "ds_read_b64_tr_b16 v[144:145], v22 offset:0\n\t" \
  "ds_read_b64_tr_b16 v[146:147], v22 offset:1024\n\t" \
  "ds_read_b128 v[132:135], v20 offset:2048\n\t" \
  "ds_read_b64_tr_b16 v[148:149], v23 offset:0\n\t" \
  "ds_read_b64_tr_b16 v[150:151], v23 offset:1024\n\t" \
  "ds_read_b128 v[136:139], v20 offset:4096\n\t" \
  "ds_read_b64_tr_b16 v[152:153], v24 offset:0\n\t" \
  "ds_read_b64_tr_b16 v[154:155], v24 offset:1024\n\t" \
  "ds_read_b128 v[140:143], v20 offset:6144\n\t" \
  "ds_read_b64_tr_b16 v[156:157], v25 offset:0\n\t" \
  "ds_read_b64_tr_b16 v[158:159], v25 offset:1024\n\t" \
  "1:\n\t" \
  "s_waitcnt lgkmcnt(0)\n\t" \
  "v_mfma_f32_16x16x32_bf16 v[64:67], v[144:147], v[128:131], v[64:67]\n\t" \
  "ds_read_b128 v[32:35], v21 offset:0\n\t" \
  "v_mfma_f32_16x16x32_bf16 v[68:71], v[148:151], v[128:131], v[68:71]\n\t" \
  "ds_read_b64_tr_b16 v[48:49], v22 offset:8192\n\t" \
  "v_mfma_f32_16x16x32_bf16 v[72:75], v[152:155], v[128:131], v[72:75]\n\t" \
  "ds_read_b64_tr_b16 v[50:51], v22 offset:9216\n\t" \
  "v_mfma_f32_16x16x32_bf16 v[76:79], v[156:159], v[128:131], v[76:79]\n\t" \
  "ds_read_b128 v[36:39], v21 offset:2048\n\t" \
  "v_mfma_f32_16x16x32_bf16 v[92:95], v[156:159], v[132:135], v[92:95]\n\t" \
  "ds_read_b64_tr_b16 v[52:53], v23 offset:8192\n\t" \
  "v_mfma_f32_16x16x32_bf16 v[88:91], v[152:155], v[132:135], v[88:91]\n\t" \
  "ds_read_b64_tr_b16 v[54:55], v23 offset:9216\n\t" \
  "ds_read_b128 v[40:43], v21 offset:4096\n\t" \
  "v_mfma_f32_16x16x32_bf16 v[84:87], v[148:151], v[132:135], v[84:87]\n\t" \
  "ds_read_b64_tr_b16 v[56:57], v24 offset:8192\n\t" \
  "v_mfma_f32_16x16x32_bf16 v[80:83], v[144:147], v[132:135], v[80:83]\n\t" \
  "ds_read_b64_tr_b16 v[58:59], v24 offset:9216\n\t" \
  "ds_read_b128 v[44:47], v21 offset:6144\n\t" \
  "v_mfma_f32_16x16x32_bf16 v[96:99], v[144:147], v[136:139], v[96:99]\n\t" \
  "ds_read_b64_tr_b16 v[60:61], v25 offset:8192\n\t" \
  "v_mfma_f32_16x16x32_bf16 v[100:103], v[148:151], v[136:139], v[100:103]\n\t" \
  "ds_read_b64_tr_b16 v[62:63], v25 offset:9216\n\t" \
  "v_mfma_f32_16x16x32_bf16 v[104:107], v[152:155], v[136:139], v[104:107]\n\t" \
  "s_add_u32 s62, s60, 32768\n\t" \
  "s_and_b32 s62, s62, 0x1ffff\n\t" \
  "v_mfma_f32_16x16x32_bf16 v[108:111], v[156:159], v[136:139], v[108:111]\n\t" \
  "s_xor_b32 s63, s62, s60\n\t" \
  "s_mov_b32 s60, s62\n\t" \
  "v_mfma_f32_16x16x32_bf16 v[124:127], v[156:159], v[140:143], v[124:127]\n\t" \
  "v_xor_b32 v20, s63, v20\n\t" \
  "v_xor_b32 v21, s63, v21\n\t" \
  "v_mfma_f32_16x16x32_bf16 v[120:123], v[152:155], v[140:143], v[120:123]\n\t" \
  "v_xor_b32 v22, s63, v22\n\t" \
  "v_xor_b32 v23, s63, v23\n\t" \
  "v_mfma_f32_16x16x32_bf16 v[116:119], v[148:151], v[140:143], v[116:119]\n\t" \
  "v_xor_b32 v24, s63, v24\n\t" \
  "v_xor_b32 v25, s63, v25\n\t" \
  "v_mfma_f32_16x16x32_bf16 v[112:115], v[144:147], v[140:143], v[112:115]\n\t" \
  "s_add_u32 s61, s61, 1\n\t" \
  "s_cmp_eq_u32 s61, 10\n\t" \
  "s_waitcnt lgkmcnt(0)\n\t" \
  "v_mfma_f32_16x16x32_bf16 v[64:67], v[48:51], v[32:35], v[64:67]\n\t" \
  "s_barrier\n\t" \
  "s_cbranch_scc1 2f\n\t" \
  "v_mfma_f32_16x16x32_bf16 v[68:71], v[52:55], v[32:35], v[68:71]\n\t" \
  "ds_read_b128 v[128:131], v20 offset:0\n\t" \
  "v_mfma_f32_16x16x32_bf16 v[72:75], v[56:59], v[32:35], v[72:75]\n\t" \
  "ds_read_b64_tr_b16 v[144:145], v22 offset:0\n\t" \
  "v_mfma_f32_16x16x32_bf16 v[76:79], v[60:63], v[32:35], v[76:79]\n\t" \
  "ds_read_b64_tr_b16 v[146:147], v22 offset:1024\n\t" \
  "v_mfma_f32_16x16x32_bf16 v[92:95], v[60:63], v[36:39], v[92:95]\n\t" \
  "ds_read_b128 v[132:135], v20 offset:2048\n\t" \
  "v_mfma_f32_16x16x32_bf16 v[88:91], v[56:59], v[36:39], v[88:91]\n\t" \
  "ds_read_b64_tr_b16 v[148:149], v23 offset:0\n\t" \
  "v_mfma_f32_16x16x32_bf16 v[84:87], v[52:55], v[36:39], v[84:87]\n\t" \
  "ds_read_b64_tr_b16 v[150:151], v23 offset:1024\n\t" \
  "ds_read_b128 v[136:139], v20 offset:4096\n\t" \
  "v_mfma_f32_16x16x32_bf16 v[80:83], v[48:51], v[36:39], v[80:83]\n\t" \
  "ds_read_b64_tr_b16 v[152:153], v24 offset:0\n\t" \
  "v_mfma_f32_16x16x32_bf16 v[96:99], v[48:51], v[40:43], v[96:99]\n\t" \
  "ds_read_b64_tr_b16 v[154:155], v24 offset:1024\n\t" \
  "ds_read_b128 v[140:143], v20 offset:6144\n\t" \
  "v_mfma_f32_16x16x32_bf16 v[100:103], v[52:55], v[40:43], v[100:103]\n\t" \
  "ds_read_b64_tr_b16 v[156:157], v25 offset:0\n\t" \
  "v_mfma_f32_16x16x32_bf16 v[104:107], v[56:59], v[40:43], v[104:107]\n\t" \
  "ds_read_b64_tr_b16 v[158:159], v25 offset:1024\n\t" \
  "v_mfma_f32_16x16x32_bf16 v[108:111], v[60:63], v[40:43], v[108:111]\n\t" \
  "v_mfma_f32_16x16x32_bf16 v[124:127], v[60:63], v[44:47], v[124:127]\n\t" \
  "v_mfma_f32_16x16x32_bf16 v[120:123], v[56:59], v[44:47], v[120:123]\n\t" \
  "v_mfma_f32_16x16x32_bf16 v[116:119], v[52:55], v[44:47], v[116:119]\n\t" \
  "v_mfma_f32_16x16x32_bf16 v[112:115], v[48:51], v[44:47], v[112:115]\n\t" \
  "s_branch 1b\n\t" \
  "2:\n\t" \
  "v_mfma_f32_16x16x32_bf16 v[68:71], v[52:55], v[32:35], v[68:71]\n\t" \
  "v_mfma_f32_16x16x32_bf16 v[72:75], v[56:59], v[32:35], v[72:75]\n\t" \
  "v_mfma_f32_16x16x32_bf16 v[76:79], v[60:63], v[32:35], v[76:79]\n\t" \
  "v_mfma_f32_16x16x32_bf16 v[92:95], v[60:63], v[36:39], v[92:95]\n\t" \
  "v_mfma_f32_16x16x32_bf16 v[88:91], v[56:59], v[36:39], v[88:91]\n\t" \
  "v_mfma_f32_16x16x32_bf16 v[84:87], v[52:55], v[36:39], v[84:87]\n\t" \
  "v_mfma_f32_16x16x32_bf16 v[80:83], v[48:51], v[36:39], v[80:83]\n\t" \
  "v_mfma_f32_16x16x32_bf16 v[96:99], v[48:51], v[40:43], v[96:99]\n\t" \
  "v_mfma_f32_16x16x32_bf16 v[100:103], v[52:55], v[40:43], v[100:103]\n\t" \
  "v_mfma_f32_16x16x32_bf16 v[104:107], v[56:59], v[40:43], v[104:107]\n\t" \
  "v_mfma_f32_16x16x32_bf16 v[108:111], v[60:63], v[40:43], v[108:111]\n\t" \
  "v_mfma_f32_16x16x32_bf16 v[124:127], v[60:63], v[44:47], v[124:127]\n\t" \
  "v_mfma_f32_16x16x32_bf16 v[120:123], v[56:59], v[44:47], v[120:123]\n\t" \
  "v_mfma_f32_16x16x32_bf16 v[116:119], v[52:55], v[44:47], v[116:119]\n\t" \
  "v_mfma_f32_16x16x32_bf16 v[112:115], v[48:51], v[44:47], v[112:115]\n\t" \
  "s_branch 8f\n\t" \
  "7:\n\t" \
  "s_barrier\n\t" \
  "ds_read_b128 v[128:131], v20 offset:0\n\t" \
  "ds_read_b64_tr_b16 v[144:145], v22 offset:0\n\t" \
  "ds_read_b64_tr_b16 v[146:147], v22 offset:1024\n\t" \
  "ds_read_b128 v[132:135], v20 offset:2048\n\t" \
  "ds_read_b64_tr_b16 v[148:149], v23 offset:0\n\t" \
  "ds_read_b64_tr_b16 v[150:151], v23 offset:1024\n\t" \
  "ds_read_b128 v[136:139], v20 offset:4096\n\t" \
  "ds_read_b64_tr_b16 v[152:153], v24 offset:0\n\t" \
  "ds_read_b64_tr_b16 v[154:155], v24 offset:1024\n\t" \
  "ds_read_b128 v[140:143], v20 offset:6144\n\t" \
  "ds_read_b64_tr_b16 v[156:157], v25 offset:0\n\t" \
  "ds_read_b64_tr_b16 v[158:159], v25 offset:1024\n\t" \
  "1:\n\t" \
  "s_waitcnt lgkmcnt(0)\n\t" \
  "v_mfma_f32_16x16x32_bf16 v[64:67], v[128:131], v[144:147], v[64:67]\n\t" \
  "ds_read_b128 v[32:35], v21 offset:0\n\t" \
  "v_mfma_f32_16x16x32_bf16 v[68:71], v[132:135], v[144:147], v[68:71]\n\t" \
  "ds_read_b64_tr_b16 v[48:49], v22 offset:8192\n\t" \
  "v_mfma_f32_16x16x32_bf16 v[72:75], v[136:139], v[144:147], v[72:75]\n\t" \
  "ds_read_b64_tr_b16 v[50:51], v22 offset:9216\n\t" \
  "v_mfma_f32_16x16x32_bf16 v[76:79], v[140:143], v[144:147], v[76:79]\n\t" \
  "ds_read_b128 v[36:39], v21 offset:2048\n\t" \
  "v_mfma_f32_16x16x32_bf16 v[92:95], v[140:143], v[148:151], v[92:95]\n\t" \
  "ds_read_b64_tr_b16 v[52:53], v23 offset:8192\n\t" \
  "v_mfma_f32_16x16x32_bf16 v[88:91], v[136:139], v[148:151], v[88:91]\n\t" \
  "ds_read_b64_tr_b16 v[54:55], v23 offset:9216\n\t" \
  "ds_read_b128 v[40:43], v21 offset:4096\n\t" \
  "v_mfma_f32_16x16x32_bf16 v[84:87], v[132:135], v[148:151], v[84:87]\n\t" \
  "ds_read_b64_tr_b16 v[56:57], v24 offset:8192\n\t" \
  "v_mfma_f32_16x16x32_bf16 v[80:83], v[128:131], v[148:151], v[80:83]\n\t" \
  "ds_read_b64_tr_b16 v[58:59], v24 offset:9216\n\t" \
  "ds_read_b128 v[44:47], v21 offset:6144\n\t" \
  "v_mfma_f32_16x16x32_bf16 v[96:99], v[128:131], v[152:155], v[96:99]\n\t" \
  "ds_read_b64_tr_b16 v[60:61], v25 offset:8192\n\t" \
  "v_mfma_f32_16x16x32_bf16 v[100:103], v[132:135], v[152:155], v[100:103]\n\t" \
  "ds_read_b64_tr_b16 v[62:63], v25 offset:9216\n\t" \
  "v_mfma_f32_16x16x32_bf16 v[104:107], v[136:139], v[152:155], v[104:107]\n\t" \
  "s_add_u32 s62, s60, 32768\n\t" \
  "s_and_b32 s62, s62, 0x1ffff\n\t" \
  "v_mfma_f32_16x16x32_bf16 v[108:111], v[140:143], v[152:155], v[108:111]\n\t" \
  "s_xor_b32 s63, s62, s60\n\t" \
  "s_mov_b32 s60, s62\n\t" \
  "v_mfma_f32_16x16x32_bf16 v[124:127], v[140:143], v[156:159], v[124:127]\n\t" \
  "v_xor_b32 v20, s63, v20\n\t" \
  "v_xor_b32 v21, s63, v21\n\t" \
  "v_mfma_f32_16x16x32_bf16 v[120:123], v[136:139], v[156:159], v[120:123]\n\t" \
  "v_xor_b32 v22, s63, v22\n\t" \
  "v_xor_b32 v23, s63, v23\n\t" \
  "v_mfma_f32_16x16x32_bf16 v[116:119], v[132:135], v[156:159], v[116:119]\n\t" \
  "v_xor_b32 v24, s63, v24\n\t" \
  "v_xor_b32 v25, s63, v25\n\t" \
  "v_mfma_f32_16x16x32_bf16 v[112:115], v[128:131], v[156:159], v[112:115]\n\t" \
  "s_add_u32 s61, s61, 1\n\t" \
  "s_cmp_eq_u32 s61, 10\n\t" \
  "s_waitcnt lgkmcnt(0)\n\t" \
  "v_mfma_f32_16x16x32_bf16 v[64:67], v[32:35], v[48:51], v[64:67]\n\t" \
  "s_barrier\n\t" \
  "s_cbranch_scc1 2f\n\t" \
  "v_mfma_f32_16x16x32_bf16 v[68:71], v[36:39], v[48:51], v[68:71]\n\t" \
  "ds_read_b128 v[128:131], v20 offset:0\n\t" \
  "v_mfma_f32_16x16x32_bf16 v[72:75], v[40:43], v[48:51], v[72:75]\n\t" \
  "ds_read_b64_tr_b16 v[144:145], v22 offset:0\n\t" \
  "v_mfma_f32_16x16x32_bf16 v[76:79], v[44:47], v[48:51], v[76:79]\n\t" \
  "ds_read_b64_tr_b16 v[146:147], v22 offset:1024\n\t" \
  "v_mfma_f32_16x16x32_bf16 v[92:95], v[44:47], v[52:55], v[92:95]\n\t" \
  "ds_read_b128 v[132:135], v20 offset:2048\n\t" \
  "v_mfma_f32_16x16x32_bf16 v[88:91], v[40:43], v[52:55], v[88:91]\n\t" \
  "ds_read_b64_tr_b16 v[148:149], v23 offset:0\n\t" \
  "v_mfma_f32_16x16x32_bf16 v[84:87], v[36:39], v[52:55], v[84:87]\n\t" \
  "ds_read_b64_tr_b16 v[150:151], v23 offset:1024\n\t" \
  "ds_read_b128 v[136:139], v20 offset:4096\n\t" \
  "v_mfma_f32_16x16x32_bf16 v[80:83], v[32:35], v[52:55], v[80:83]\n\t" \
  "ds_read_b64_tr_b16 v[152:153], v24 offset:0\n\t" \
  "v_mfma_f32_16x16x32_bf16 v[96:99], v[32:35], v[56:59], v[96:99]\n\t" \
  "ds_read_b64_tr_b16 v[154:155], v24 offset:1024\n\t" \
  "ds_read_b128 v[140:143], v20 offset:6144\n\t" \
  "v_mfma_f32_16x16x32_bf16 v[100:103], v[36:39], v[56:59], v[100:103]\n\t" \
  "ds_read_b64_tr_b16 v[156:157], v25 offset:0\n\t" \
  "v_mfma_f32_16x16x32_bf16 v[104:107], v[40:43], v[56:59], v[104:107]\n\t" \
  "ds_read_b64_tr_b16 v[158:159], v25 offset:1024\n\t" \
  "v_mfma_f32_16x16x32_bf16 v[108:111], v[44:47], v[56:59], v[108:111]\n\t" \
  "v_mfma_f32_16x16x32_bf16 v[124:127], v[44:47], v[60:63], v[124:127]\n\t" \
  "v_mfma_f32_16x16x32_bf16 v[120:123], v[40:43], v[60:63], v[120:123]\n\t" \
  "v_mfma_f32_16x16x32_bf16 v[116:119], v[36:39], v[60:63], v[116:119]\n\t" \
  "v_mfma_f32_16x16x32_bf16 v[112:115], v[32:35], v[60:63], v[112:115]\n\t" \
  "s_branch 1b\n\t" \
  "2:\n\t" \
  "v_mfma_f32_16x16x32_bf16 v[68:71], v[36:39], v[48:51], v[68:71]\n\t" \
  "v_mfma_f32_16x16x32_bf16 v[72:75], v[40:43], v[48:51], v[72:75]\n\t" \
  "v_mfma_f32_16x16x32_bf16 v[76:79], v[44:47], v[48:51], v[76:79]\n\t" \
  "v_mfma_f32_16x16x32_bf16 v[92:95], v[44:47], v[52:55], v[92:95]\n\t" \
  "v_mfma_f32_16x16x32_bf16 v[88:91], v[40:43], v[52:55], v[88:91]\n\t" \
  "v_mfma_f32_16x16x32_bf16 v[84:87], v[36:39], v[52:55], v[84:87]\n\t" \
  "v_mfma_f32_16x16x32_bf16 v[80:83], v[32:35], v[52:55], v[80:83]\n\t" \
  "v_mfma_f32_16x16x32_bf16 v[96:99], v[32:35], v[56:59], v[96:99]\n\t" \
  "v_mfma_f32_16x16x32_bf16 v[100:103], v[36:39], v[56:59], v[100:103]\n\t" \
  "v_mfma_f32_16x16x32_bf16 v[104:107], v[40:43], v[56:59], v[104:107]\n\t" \
  "v_mfma_f32_16x16x32_bf16 v[108:111], v[44:47], v[56:59], v[108:111]\n\t" \
  "v_mfma_f32_16x16x32_bf16 v[124:127], v[44:47], v[60:63], v[124:127]\n\t" \
  "v_mfma_f32_16x16x32_bf16 v[120:123], v[40:43], v[60:63], v[120:123]\n\t" \
  "v_mfma_f32_16x16x32_bf16 v[116:119], v[36:39], v[60:63], v[116:119]\n\t" \
  "v_mfma_f32_16x16x32_bf16 v[112:115], v[32:35], v[60:63], v[112:115]\n\t" \
  "8:\n\t" \
  "s_setprio 3\n\t" \
  "s_waitcnt vmcnt(0)\n\t" \
  "s_cmp_eq_u32 s45, 0\n\t" \
  "s_cbranch_scc1 3f\n\t" \
  "s_mov_b32 s62, s45\n\t" \
  "s_mov_b32 s63, s45\n\t" \
  "v_pk_mul_f32 v[64:65], v[64:65], s[62:63]\n\t" \
  "v_pk_mul_f32 v[66:67], v[66:67], s[62:63]\n\t" \
  "v_pk_mul_f32 v[68:69], v[68:69], s[62:63]\n\t" \
  "v_pk_mul_f32 v[70:71], v[70:71], s[62:63]\n\t" \
  "v_pk_mul_f32 v[72:73], v[72:73], s[62:63]\n\t" \
  "v_pk_mul_f32 v[74:75], v[74:75], s[62:63]\n\t" \
  "v_pk_mul_f32 v[76:77], v[76:77], s[62:63]\n\t" \
  "v_pk_mul_f32 v[78:79], v[78:79], s[62:63]\n\t" \
  "v_pk_mul_f32 v[80:81], v[80:81], s[62:63]\n\t" \
  "v_pk_mul_f32 v[82:83], v[82:83], s[62:63]\n\t" \
  "v_pk_mul_f32 v[84:85], v[84:85], s[62:63]\n\t" \
  "v_pk_mul_f32 v[86:87], v[86:87], s[62:63]\n\t" \
  "v_pk_mul_f32 v[88:89], v[88:89], s[62:63]\n\t" \
  "v_pk_mul_f32 v[90:91], v[90:91], s[62:63]\n\t" \
  "v_pk_mul_f32 v[92:93], v[92:93], s[62:63]\n\t" \
  "v_pk_mul_f32 v[94:95], v[94:95], s[62:63]\n\t" \
  "v_pk_mul_f32 v[96:97], v[96:97], s[62:63]\n\t" \
  "v_pk_mul_f32 v[98:99], v[98:99], s[62:63]\n\t" \
  "v_pk_mul_f32 v[100:101], v[100:101], s[62:63]\n\t" \
  "v_pk_mul_f32 v[102:103], v[102:103], s[62:63]\n\t" \
  "v_pk_mul_f32 v[104:105], v[104:105], s[62:63]\n\t" \
  "v_pk_mul_f32 v[106:107], v[106:107], s[62:63]\n\t" \
  "v_pk_mul_f32 v[108:109], v[108:109], s[62:63]\n\t" \
  "v_pk_mul_f32 v[110:111], v[110:111], s[62:63]\n\t" \
  "v_pk_mul_f32 v[112:113], v[112:113], s[62:63]\n\t" \
  "v_pk_mul_f32 v[114:115], v[114:115], s[62:63]\n\t" \
  "v_pk_mul_f32 v[116:117], v[116:117], s[62:63]\n\t" \
  "v_pk_mul_f32 v[118:119], v[118:119], s[62:63]\n\t" \
  "v_pk_mul_f32 v[120:121], v[120:121], s[62:63]\n\t" \
  "v_pk_mul_f32 v[122:123], v[122:123], s[62:63]\n\t" \
  "v_pk_mul_f32 v[124:125], v[124:125], s[62:63]\n\t" \
  "v_pk_mul_f32 v[126:127], v[126:127], s[62:63]\n\t" \
  "3:\n\t" \
  "s_cmp_eq_u32 s64, 0\n\t" \
  "v_cvt_pk_bf16_f32 v64, v64, v65\n\t" \
  "v_cvt_pk_bf16_f32 v65, v66, v67\n\t" \
  "v_cvt_pk_bf16_f32 v66, v68, v69\n\t" \
  "v_cvt_pk_bf16_f32 v67, v70, v71\n\t" \
  "v_cvt_pk_bf16_f32 v72, v72, v73\n\t" \
  "v_cvt_pk_bf16_f32 v73, v74, v75\n\t" \
  "v_cvt_pk_bf16_f32 v74, v76, v77\n\t" \
  "v_cvt_pk_bf16_f32 v75, v78, v79\n\t" \
  "v_permlane16_swap_b32 v64, v66\n\t" \
  "v_permlane16_swap_b32 v65, v67\n\t" \
  "v_cvt_pk_bf16_f32 v80, v80, v81\n\t" \
  "v_cvt_pk_bf16_f32 v81, v82, v83\n\t" \
  "v_cvt_pk_bf16_f32 v82, v84, v85\n\t" \
  "v_cvt_pk_bf16_f32 v83, v86, v87\n\t" \
  "v_permlane16_swap_b32 v72, v74\n\t" \
  "v_permlane16_swap_b32 v73, v75\n\t" \
  "v_cvt_pk_bf16_f32 v88, v88, v89\n\t" \
  "v_cvt_pk_bf16_f32 v89, v90, v91\n\t" \
  "v_cvt_pk_bf16_f32 v90, v92, v93\n\t" \
  "v_cvt_pk_bf16_f32 v91, v94, v95\n\t" \
  "v_permlane16_swap_b32 v80, v82\n\t" \
  "v_permlane16_swap_b32 v81, v83\n\t" \
  "v_cvt_pk_bf16_f32 v96, v96, v97\n\t" \
  "v_cvt_pk_bf16_f32 v97, v98, v99\n\t" \
  "v_cvt_pk_bf16_f32 v98, v100, v101\n\t" \
  "v_cvt_pk_bf16_f32 v99, v102, v103\n\t" \
  "v_permlane16_swap_b32 v88, v90\n\t" \
  "v_permlane16_swap_b32 v89, v91\n\t" \
  "v_cvt_pk_bf16_f32 v104, v104, v105\n\t" \
  "v_cvt_pk_bf16_f32 v105, v106, v107\n\t" \
  "v_cvt_pk_bf16_f32 v106, v108, v109\n\t" \
  "v_cvt_pk_bf16_f32 v107, v110, v111\n\t" \
  "v_permlane16_swap_b32 v96, v98\n\t" \
  "v_permlane16_swap_b32 v97, v99\n\t" \
  "v_cvt_pk_bf16_f32 v112, v112, v113\n\t" \
  "v_cvt_pk_bf16_f32 v113, v114, v115\n\t" \
  "v_cvt_pk_bf16_f32 v114, v116, v117\n\t" \
  "v_cvt_pk_bf16_f32 v115, v118, v119\n\t" \
  "v_permlane16_swap_b32 v104, v106\n\t" \
  "v_permlane16_swap_b32 v105, v107\n\t" \
  "v_cvt_pk_bf16_f32 v120, v120, v121\n\t" \
  "v_cvt_pk_bf16_f32 v121, v122, v123\n\t" \
  "v_cvt_pk_bf16_f32 v122, v124, v125\n\t" \
  "v_cvt_pk_bf16_f32 v123, v126, v127\n\t" \
  "v_permlane16_swap_b32 v112, v114\n\t" \
  "v_permlane16_swap_b32 v113, v115\n\t" \
  "s_nop 1\n\t" \
  "v_permlane16_swap_b32 v120, v122\n\t" \
  "v_permlane16_swap_b32 v121, v123\n\t" \
  "s_cbranch_scc0 4f\n\t" \
  "s_barrier\n\t" \
  "4:\n\t" \
  "buffer_store_dwordx4 v[64:67], v26, s[40:43], 0 offen offset:0 sc1\n\t" \
  "buffer_store_dwordx4 v[72:75], v26, s[40:43], s44 offen offset:0 sc1\n\t" \
  "buffer_store_dwordx4 v[80:83], v26, s[40:43], 0 offen offset:256 sc1\n\t" \
  "buffer_store_dwordx4 v[88:91], v26, s[40:43], s44 offen offset:256 sc1\n\t" \
  "buffer_store_dwordx4 v[96:99], v26, s[40:43], 0 offen offset:512 sc1\n\t" \
  "buffer_store_dwordx4 v[104:107], v26, s[40:43], s44 offen offset:512 sc1\n\t" \
  "buffer_store_dwordx4 v[112:115], v26, s[40:43], 0 offen offset:768 sc1\n\t" \
  "buffer_store_dwordx4 v[120:123], v26, s[40:43], s44 offen offset:768 sc1\n\t" \
  "s_nop 1\n\t" \
  ""

#define ATTN_ASM \
  "v_add_u32 v124, s45, v0\n\t" \
  "v_add_u32 v125, s45, v1\n\t" \
  "v_add_u32 v126, s46, v2\n\t" \
  "v_add_u32 v12, s47, v0\n\t" \
  "v_add_u32 v14, s47, v1\n\t" \
  "v_xor_b32 v13, 512, v12\n\t" \
  "v_xor_b32 v15, 512, v14\n\t" \
  "v_mov_b64 v[104:105], 0\n\t" \
  "v_mov_b64 v[106:107], 0\n\t" \
  "s_mov_b32 s60, 0x3f80\n\t" \
  "s_mov_b32 s61, 0\n\t" \
  "v_mov_b64 v[116:117], s[60:61]\n\t" \
  "v_mov_b64 v[118:119], s[60:61]\n\t" \
  "v_mov_b64 v[120:121], s[60:61]\n\t" \
  "v_mov_b64 v[122:123], s[60:61]\n\t" \
  "v_mov_b64 v[16:17], 0\n\t" \
  "v_mov_b64 v[18:19], 0\n\t" \
  "v_mov_b64 v[20:21], 0\n\t" \
  "v_mov_b64 v[22:23], 0\n\t" \
  "v_mov_b64 v[24:25], 0\n\t" \
  "v_mov_b64 v[26:27], 0\n\t" \
  "v_mov_b64 v[28:29], 0\n\t" \
  "v_mov_b64 v[30:31], 0\n\t" \
  "s_mov_b32 s60, 0x3f803f80\n\t" \
  "s_mov_b32 s61, 0x3f803f80\n\t" \
  "s_mov_b32 exec_lo, 0x100000\n\t" \
  "s_mov_b32 exec_hi, 0x100000\n\t" \
  "v_mov_b64 v[16:17], s[60:61]\n\t" \
  "v_mov_b64 v[18:19], s[60:61]\n\t" \
  "v_mov_b64 v[20:21], s[60:61]\n\t" \
  "v_mov_b64 v[22:23], s[60:61]\n\t" \
  "v_mov_b64 v[24:25], s[60:61]\n\t" \
  "v_mov_b64 v[26:27], s[60:61]\n\t" \
  "v_mov_b64 v[28:29], s[60:61]\n\t" \
  "v_mov_b64 v[30:31], s[60:61]\n\t" \
  "s_mov_b64 exec, -1\n\t" \
  "s_mov_b32 s60, 0xfffff\n\t" \
  "s_mov_b32 s61, 0xfffff\n\t" \
  "s_mov_b32 s55, 0\n\t" \
  "s_mov_b32 s62, 0\n\t" \
  "s_mov_b32 s63, 0\n\t" \
  "s_lshr_b32 s47, s45, 11\n\t" \
  "s_add_u32 s47, s47, 5\n\t" \
  "s_setprio 2\n\t" \
  "s_waitcnt vmcnt(5)\n\t" \
  "s_barrier\n\t" \
  "ds_read_b128 v[112:115], v0\n\t" \
  "ds_read_b128 v[96:99], v12\n\t" \
  "ds_read_b128 v[100:103], v13\n\t" \
  "s_mov_b32 exec_hi, 0\n\t" \
  "ds_read_b64 v[120:121], v1 offset:8192\n\t" \
  "ds_read_b64 v[104:105], v14 offset:8192\n\t" \
  "ds_read_b64 v[106:107], v15 offset:8192\n\t" \
  "s_mov_b32 exec_hi, -1\n\t" \
  "ds_read_b128 v[108:111], v124 offset:0\n\t" \
  "s_mov_b32 exec_hi, 0\n\t" \
  "ds_read2st64_b64 v[116:119], v125 offset0:16 offset1:17\n\t" \
  "s_mov_b32 exec_hi, -1\n\t" \
  "s_mov_b64 exec, s[60:61]\n\t" \
  "ds_read_b128 v[16:19], v126 offset:0\n\t" \
  "ds_read_b128 v[20:23], v126 offset:640\n\t" \
  "s_mov_b64 exec, -1\n\t" \
  "v_mov_b64 v[64:65], 0\n\t" \
  "v_mov_b64 v[66:67], 0\n\t" \
  "v_mov_b64 v[68:69], 0\n\t" \
  "v_mov_b64 v[70:71], 0\n\t" \
  "v_mov_b64 v[72:73], 0\n\t" \
  "v_mov_b64 v[74:75], 0\n\t" \
  "v_mov_b64 v[76:77], 0\n\t" \
  "v_mov_b64 v[78:79], 0\n\t" \
  "s_waitcnt lgkmcnt(7)\n\t" \
  "v_mfma_f32_32x32x16_bf16 v[32:47], v[112:115], v[96:99], 0\n\t" \
  "v_mov_b64 v[80:81], 0\n\t" \
  "v_mov_b64 v[82:83], 0\n\t" \
  "v_mov_b64 v[84:85], 0\n\t" \
  "v_mov_b64 v[86:87], 0\n\t" \
  "v_mfma_f32_32x32x16_bf16 v[48:63], v[112:115], v[100:103], 0\n\t" \
  "v_mov_b64 v[88:89], 0\n\t" \
  "v_mov_b64 v[90:91], 0\n\t" \
  "v_mov_b64 v[92:93], 0\n\t" \
  "v_mov_b64 v[94:95], 0\n\t" \
  "s_waitcnt lgkmcnt(4)\n\t" \
  "v_mfma_f32_32x32x8_bf16 v[32:47], v[120:121], v[104:105], v[32:47]\n\t" \
  "v_mov_b32 v127, 0xdddddddd\n\t" \
  "v_mfma_f32_32x32x8_bf16 v[48:63], v[120:121], v[106:107], v[48:63]\n\t" \
  "ds_read_b128 v[112:115], v124 offset:512\n\t" \
  "s_mov_b32 s66, 0x26800000\n\t" \
  "s_mov_b32 s67, 0x7b800000\n\t" \
  "s_nop 3\n\t" \
  "v_max3_f32 v12, v32, v33, v34\n\t" \
  "v_max3_f32 v12, v12, v35, v36\n\t" \
  "v_max3_f32 v12, v12, v37, v38\n\t" \
  "v_max3_f32 v12, v12, v39, v40\n\t" \
  "v_max3_f32 v12, v12, v41, v42\n\t" \
  "v_max3_f32 v12, v12, v43, v44\n\t" \
  "v_max3_f32 v12, v12, v45, v46\n\t" \
  "v_max_f32 v12, v12, v47\n\t" \
  "v_max3_f32 v13, v48, v49, v50\n\t" \
  "v_max3_f32 v13, v13, v51, v52\n\t" \
  "v_max3_f32 v13, v13, v53, v54\n\t" \
  "v_max3_f32 v13, v13, v55, v56\n\t" \
  "v_max3_f32 v13, v13, v57, v58\n\t" \
  "v_max3_f32 v13, v13, v59, v60\n\t" \
  "v_max3_f32 v13, v13, v61, v62\n\t" \
  "v_max_f32 v13, v13, v63\n\t" \
  "s_waitcnt lgkmcnt(0)\n\t" \
  "v_mfma_f32_32x32x16_bf16 v[32:47], v[108:111], v[96:99], 0\n\t" \
  "v_mov_b32 v14, v12\n\t" \
  "v_mov_b32 v15, v13\n\t" \
  "s_nop 1\n\t" \
  "v_permlane32_swap_b32 v12, v14\n\t" \
  "v_permlane32_swap_b32 v13, v15\n\t" \
  "s_nop 0\n\t" \
  "v_max_f32 v12, v12, v14\n\t" \
  "v_max_f32 v13, v13, v15\n\t" \
  "v_add_f32 v12, 0x41f00000, v12\n\t" \
  "v_add_f32 v13, 0x41f00000, v13\n\t" \
  "s_mov_b32 exec_lo, 0\n\t" \
  "v_cvt_pk_bf16_f32 v104, -v12, 0\n\t" \
  "v_cvt_pk_bf16_f32 v106, -v13, 0\n\t" \
  "s_mov_b32 exec_lo, -1\n\t" \
  "s_nop 0\n\t" \
  "v_mfma_f32_32x32x8_bf16 v[32:47], v[116:117], v[104:105], v[32:47]\n\t" \
  "v_mov_b64 v[56:57], 0\n\t" \
  "v_mov_b64 v[58:59], 0\n\t" \
  "v_mov_b64 v[60:61], 0\n\t" \
  "v_mov_b64 v[62:63], 0\n\t" \
  "s_setprio 1\n\t" \
  "s_nop 1\n\t" \
  "s_branch 1f\n\t" \
  "7:\n\t" \
  "global_load_dwordx4 v[128:131], v4, s[58:59] offset:0\n\t" \
  "global_load_dwordx4 v[132:135], v4, s[58:59] offset:1024\n\t" \
  "global_load_dwordx4 v[136:139], v4, s[58:59] offset:2048\n\t" \
  "global_load_dwordx4 v[140:143], v4, s[58:59] offset:3072\n\t" \
  "s_and_b32 s64, s52, 0xffff\n\t" \
  "s_lshr_b32 s65, s52, 16\n\t" \
  "v_add_u32 v10, s64, v4\n\t" \
  "v_lshrrev_b32 v11, 1, v4\n\t" \
  "v_add_u32 v11, s65, v11\n\t" \
  "global_load_dwordx4 v[160:163], v10, s[58:59]\n\t" \
  "global_load_dwordx2 v[164:165], v11, s[58:59]\n\t" \
  "s_branch 8f\n\t" \
  "1:\n\t" \
  "v_exp_f32 v32, v32\n\t" \
  "v_exp_f32 v33, v33\n\t" \
  "v_smfmac_f32_32x32x32_bf16 v[80:95], v[28:31], v[56:63], v127\n\t" \
  "s_waitcnt lgkmcnt(0)\n\t" \
  "s_mov_b64 exec, s[60:61]\n\t" \
  "ds_read_b128 v[24:27], v126 offset:1280\n\t" \
  "ds_read_b128 v[28:31], v126 offset:1920\n\t" \
  "s_mov_b64 exec, -1\n\t" \
  "v_exp_f32 v34, v34\n\t" \
  "v_exp_f32 v35, v35\n\t" \
  "v_exp_f32 v36, v36\n\t" \
  "v_exp_f32 v37, v37\n\t" \
  "v_mfma_f32_32x32x8_bf16 v[48:63], v[116:117], v[106:107], 0\n\t" \
  "v_exp_f32 v38, v38\n\t" \
  "v_exp_f32 v39, v39\n\t" \
  "v_exp_f32 v40, v40\n\t" \
  "v_exp_f32 v41, v41\n\t" \
  "v_mfma_f32_32x32x16_bf16 v[48:63], v[108:111], v[100:103], v[48:63]\n\t" \
  "ds_read_b128 v[108:111], v124 offset:1024\n\t" \
  "s_mov_b32 exec_hi, 0\n\t" \
  "ds_read2st64_b64 v[120:123], v125 offset0:18 offset1:19\n\t" \
  "s_mov_b32 exec_hi, -1\n\t" \
  "v_exp_f32 v42, v42\n\t" \
  "v_exp_f32 v43, v43\n\t" \
  "v_exp_f32 v44, v44\n\t" \
  "v_exp_f32 v45, v45\n\t" \
  "v_smfmac_f32_32x32x32_bf16 v[64:79], v[16:19], v[32:39], v127\n\t" \
  "v_exp_f32 v46, v46\n\t" \
  "v_exp_f32 v47, v47\n\t" \
  "v_exp_f32 v48, v48\n\t" \
  "v_exp_f32 v49, v49\n\t" \
  "v_smfmac_f32_32x32x32_bf16 v[64:79], v[20:23], v[40:47], v127\n\t" \
  "v_exp_f32 v50, v50\n\t" \
  "v_exp_f32 v51, v51\n\t" \
  "v_exp_f32 v52, v52\n\t" \
  "v_exp_f32 v53, v53\n\t" \
  "v_mfma_f32_32x32x8_bf16 v[32:47], v[118:119], v[104:105], 0\n\t" \
  "v_exp_f32 v54, v54\n\t" \
  "v_exp_f32 v55, v55\n\t" \
  "v_exp_f32 v56, v56\n\t" \
  "v_exp_f32 v57, v57\n\t" \
  "v_mfma_f32_32x32x16_bf16 v[32:47], v[112:115], v[96:99], v[32:47]\n\t" \
  "v_exp_f32 v58, v58\n\t" \
  "v_exp_f32 v59, v59\n\t" \
  "v_exp_f32 v60, v60\n\t" \
  "v_exp_f32 v61, v61\n\t" \
  "v_smfmac_f32_32x32x32_bf16 v[80:95], v[16:19], v[48:55], v127\n\t" \
  "v_exp_f32 v62, v62\n\t" \
  "v_exp_f32 v63, v63\n\t" \
  "v_exp_f32 v32, v32\n\t" \
  "v_exp_f32 v33, v33\n\t" \
  "v_smfmac_f32_32x32x32_bf16 v[80:95], v[20:23], v[56:63], v127\n\t" \
  "s_waitcnt lgkmcnt(0)\n\t" \
  "s_mov_b64 exec, s[60:61]\n\t" \
  "ds_read_b128 v[16:19], v126 offset:2560\n\t" \
  "ds_read_b128 v[20:23], v126 offset:3200\n\t" \
  "s_mov_b64 exec, -1\n\t" \
  "v_exp_f32 v34, v34\n\t" \
  "v_exp_f32 v35, v35\n\t" \
  "v_exp_f32 v36, v36\n\t" \
  "v_exp_f32 v37, v37\n\t" \
  "v_mfma_f32_32x32x8_bf16 v[48:63], v[118:119], v[106:107], 0\n\t" \
  "v_exp_f32 v38, v38\n\t" \
  "v_exp_f32 v39, v39\n\t" \
  "v_exp_f32 v40, v40\n\t" \
  "v_exp_f32 v41, v41\n\t" \
  "v_mfma_f32_32x32x16_bf16 v[48:63], v[112:115], v[100:103], v[48:63]\n\t" \
  "ds_read_b128 v[112:115], v124 offset:1536\n\t" \
  "s_setprio 0\n\t" \
  "v_exp_f32 v42, v42\n\t" \
  "v_exp_f32 v43, v43\n\t" \
  "v_exp_f32 v44, v44\n\t" \
  "v_exp_f32 v45, v45\n\t" \
  "v_smfmac_f32_32x32x32_bf16 v[64:79], v[24:27], v[32:39], v127\n\t" \
  "v_exp_f32 v46, v46\n\t" \
  "v_exp_f32 v47, v47\n\t" \
  "v_exp_f32 v48, v48\n\t" \
  "v_exp_f32 v49, v49\n\t" \
  "v_smfmac_f32_32x32x32_bf16 v[64:79], v[28:31], v[40:47], v127\n\t" \
  "v_exp_f32 v50, v50\n\t" \
  "v_exp_f32 v51, v51\n\t" \
  "v_exp_f32 v52, v52\n\t" \
  "v_exp_f32 v53, v53\n\t" \
  "v_mfma_f32_32x32x8_bf16 v[32:47], v[120:121], v[104:105], 0\n\t" \
  "v_exp_f32 v54, v54\n\t" \
  "v_exp_f32 v55, v55\n\t" \
  "v_exp_f32 v56, v56\n\t" \
  "v_exp_f32 v57, v57\n\t" \
  "v_mfma_f32_32x32x16_bf16 v[32:47], v[108:111], v[96:99], v[32:47]\n\t" \
  "v_exp_f32 v58, v58\n\t" \
  "v_exp_f32 v59, v59\n\t" \
  "v_exp_f32 v60, v60\n\t" \
  "v_exp_f32 v61, v61\n\t" \
  "v_smfmac_f32_32x32x32_bf16 v[80:95], v[24:27], v[48:55], v127\n\t" \
  "v_exp_f32 v62, v62\n\t" \
  "v_exp_f32 v63, v63\n\t" \
  "v_exp_f32 v32, v32\n\t" \
  "v_exp_f32 v33, v33\n\t" \
  "v_smfmac_f32_32x32x32_bf16 v[80:95], v[28:31], v[56:63], v127\n\t" \
  "s_waitcnt lgkmcnt(0)\n\t" \
  "s_mov_b64 exec, s[60:61]\n\t" \
  "ds_read_b128 v[24:27], v126 offset:3840\n\t" \
  "ds_read_b128 v[28:31], v126 offset:4480\n\t" \
  "s_mov_b64 exec, -1\n\t" \
  "v_exp_f32 v34, v34\n\t" \
  "v_exp_f32 v35, v35\n\t" \
  "v_exp_f32 v36, v36\n\t" \
  "v_exp_f32 v37, v37\n\t" \
  "v_mfma_f32_32x32x8_bf16 v[48:63], v[120:121], v[106:107], 0\n\t" \
  "s_mov_b32 s57, 22528\n\t" \
  "s_cmp_eq_u32 s62, 45056\n\t" \
  "s_cselect_b32 s57, 4294922240, s57\n\t" \
  "s_add_u32 s62, s62, s57\n\t" \
  "v_add_u32 v124, s57, v124\n\t" \
  "v_add_u32 v125, s57, v125\n\t" \
  "v_exp_f32 v38, v38\n\t" \
  "v_exp_f32 v39, v39\n\t" \
  "v_exp_f32 v40, v40\n\t" \
  "v_exp_f32 v41, v41\n\t" \
  "v_mfma_f32_32x32x16_bf16 v[48:63], v[108:111], v[100:103], v[48:63]\n\t" \
  "s_waitcnt vmcnt(0)\n\t" \
  "s_barrier\n\t" \
  "ds_read_b128 v[108:111], v124 offset:0\n\t" \
  "s_mov_b32 exec_hi, 0\n\t" \
  "ds_read2st64_b64 v[116:119], v125 offset0:16 offset1:17\n\t" \
  "s_mov_b32 exec_hi, -1\n\t" \
  "s_setprio 2\n\t" \
  "v_exp_f32 v42, v42\n\t" \
  "v_exp_f32 v43, v43\n\t" \
  "v_exp_f32 v44, v44\n\t" \
  "v_exp_f32 v45, v45\n\t" \
  "v_smfmac_f32_32x32x32_bf16 v[64:79], v[16:19], v[32:39], v127\n\t" \
  "s_cmp_eq_u32 s55, s47\n\t" \
  "s_cbranch_scc1 7b\n\t" \
  "8:\n\t" \
  "v_exp_f32 v46, v46\n\t" \
  "v_exp_f32 v47, v47\n\t" \
  "v_exp_f32 v48, v48\n\t" \
  "v_exp_f32 v49, v49\n\t" \
  "v_smfmac_f32_32x32x32_bf16 v[64:79], v[20:23], v[40:47], v127\n\t" \
  "v_exp_f32 v50, v50\n\t" \
  "v_exp_f32 v51, v51\n\t" \
  "v_exp_f32 v52, v52\n\t" \
  "v_exp_f32 v53, v53\n\t" \
  "v_mfma_f32_32x32x8_bf16 v[32:47], v[122:123], v[104:105], 0\n\t" \
  "v_exp_f32 v54, v54\n\t" \
  "v_exp_f32 v55, v55\n\t" \
  "v_exp_f32 v56, v56\n\t" \
  "v_exp_f32 v57, v57\n\t" \
  "v_mfma_f32_32x32x16_bf16 v[32:47], v[112:115], v[96:99], v[32:47]\n\t" \
  "v_exp_f32 v58, v58\n\t" \
  "v_exp_f32 v59, v59\n\t" \
  "v_exp_f32 v60, v60\n\t" \
  "v_exp_f32 v61, v61\n\t" \
  "v_smfmac_f32_32x32x32_bf16 v[80:95], v[16:19], v[48:55], v127\n\t" \
  "v_exp_f32 v62, v62\n\t" \
  "v_exp_f32 v63, v63\n\t" \
  "v_exp_f32 v32, v32\n\t" \
  "v_exp_f32 v33, v33\n\t" \
  "v_smfmac_f32_32x32x32_bf16 v[80:95], v[20:23], v[56:63], v127\n\t" \
  "s_waitcnt lgkmcnt(0)\n\t" \
  "v_add_u32 v126, s57, v126\n\t" \
  "s_mov_b64 exec, s[60:61]\n\t" \
  "ds_read_b128 v[16:19], v126 offset:0\n\t" \
  "ds_read_b128 v[20:23], v126 offset:640\n\t" \
  "s_mov_b64 exec, -1\n\t" \
  "v_exp_f32 v34, v34\n\t" \
  "v_exp_f32 v35, v35\n\t" \
  "v_exp_f32 v36, v36\n\t" \
  "v_exp_f32 v37, v37\n\t" \
  "v_mfma_f32_32x32x8_bf16 v[48:63], v[122:123], v[106:107], 0\n\t" \
  "v_exp_f32 v38, v38\n\t" \
  "v_exp_f32 v39, v39\n\t" \
  "v_exp_f32 v40, v40\n\t" \
  "v_exp_f32 v41, v41\n\t" \
  "v_mfma_f32_32x32x16_bf16 v[48:63], v[112:115], v[100:103], v[48:63]\n\t" \
  "ds_read_b128 v[112:115], v124 offset:512\n\t" \
  "v_exp_f32 v42, v42\n\t" \
  "v_exp_f32 v43, v43\n\t" \
  "v_exp_f32 v44, v44\n\t" \
  "v_exp_f32 v45, v45\n\t" \
  "v_smfmac_f32_32x32x32_bf16 v[64:79], v[24:27], v[32:39], v127\n\t" \
  "v_exp_f32 v46, v46\n\t" \
  "v_exp_f32 v47, v47\n\t" \
  "v_exp_f32 v48, v48\n\t" \
  "v_exp_f32 v49, v49\n\t" \
  "v_smfmac_f32_32x32x32_bf16 v[64:79], v[28:31], v[40:47], v127\n\t" \
  "v_exp_f32 v50, v50\n\t" \
  "v_exp_f32 v51, v51\n\t" \
  "v_exp_f32 v52, v52\n\t" \
  "v_exp_f32 v53, v53\n\t" \
  "v_mfma_f32_32x32x8_bf16 v[32:47], v[116:117], v[104:105], 0\n\t" \
  "v_exp_f32 v54, v54\n\t" \
  "v_exp_f32 v55, v55\n\t" \
  "v_exp_f32 v56, v56\n\t" \
  "v_exp_f32 v57, v57\n\t" \
  "v_mfma_f32_32x32x16_bf16 v[32:47], v[108:111], v[96:99], v[32:47]\n\t" \
  "s_setprio 1\n\t" \
  "v_exp_f32 v58, v58\n\t" \
  "v_exp_f32 v59, v59\n\t" \
  "v_exp_f32 v60, v60\n\t" \
  "v_exp_f32 v61, v61\n\t" \
  "v_smfmac_f32_32x32x32_bf16 v[80:95], v[24:27], v[48:55], v127\n\t" \
  "s_add_u32 s55, s55, 1\n\t" \
  "s_cmp_lt_u32 s55, 8\n\t" \
  "v_exp_f32 v62, v62\n\t" \
  "v_exp_f32 v63, v63\n\t" \
  "s_cbranch_scc1 1b\n\t" \
  ".Lk_win:\n\t" \
  "s_waitcnt vmcnt(0) lgkmcnt(0)\n\t" \
  "v_smfmac_f32_32x32x32_bf16 v[80:95], v[28:31], v[56:63], v127\n\t" \
  "s_add_u32 s48, s48, 67584\n\t" \
  "v_add_u32 v124, s48, v3\n\t" \
  "v_add_u32 v125, s48, v1\n\t" \
  "v_add_u32 v126, s49, v2\n\t" \
  "v_add_u32 v12, s48, v0\n\t" \
  "v_mov_b32 v104, 0\n\t" \
  "v_add_u32 v14, s50, v4\n\t" \
  "v_add_u32 v15, s51, v4\n\t" \
  "ds_read_b128 v[96:99], v12\n\t" \
  "s_mov_b32 exec_hi, 0\n\t" \
  "ds_read_b64 v[104:105], v125 offset:8192\n\t" \
  "s_mov_b32 exec_hi, -1\n\t" \
  "ds_read_b128 v[108:111], v124 offset:18432\n\t" \
  "s_mov_b32 exec_hi, 0\n\t" \
  "ds_read_b64 v[116:117], v125 offset:34816\n\t" \
  "s_mov_b32 exec_hi, -1\n\t" \
  "ds_read_b128 v[112:115], v124 offset:17920\n\t" \
  "s_mov_b32 exec_hi, 0\n\t" \
  "ds_read_b64 v[118:119], v125 offset:34304\n\t" \
  "s_mov_b32 exec_hi, -1\n\t" \
  "s_mov_b64 exec, s[60:61]\n\t" \
  "ds_read_b128 v[16:19], v126 offset:10240\n\t" \
  "ds_read_b128 v[20:23], v126 offset:10880\n\t" \
  "s_mov_b64 exec, -1\n\t" \
  "ds_write_b128 v14, v[80:83] offset:0\n\t" \
  "ds_write_b128 v14, v[84:87] offset:1024\n\t" \
  "ds_write_b128 v14, v[88:91] offset:2048\n\t" \
  "s_cmp_eq_u32 s44, 1\n\t" \
  "s_waitcnt lgkmcnt(5)\n\t" \
  "s_cbranch_scc1 3f\n\t" \
  "v_mfma_f32_32x32x8_bf16 v[48:63], v[118:119], v[104:105], 0\n\t" \
  "v_mfma_f32_32x32x16_bf16 v[48:63], v[112:115], v[96:99], v[48:63]\n\t" \
  "s_branch 14f\n\t" \
  "3:\n\t" \
  "v_mfma_f32_32x32x8_bf16 v[48:63], v[116:117], v[104:105], v[128:143]\n\t" \
  "v_mfma_f32_32x32x16_bf16 v[48:63], v[108:111], v[96:99], v[48:63]\n\t" \
  "14:\n\t" \
  "v_mfma_f32_32x32x16_bf16 v[32:47], v[108:111], v[96:99], v[128:143]\n\t" \
  "s_setprio 0\n\t" \
  "s_nop 3\n\t" \
  "v_max3_f32 v12, v48, v49, v50\n\t" \
  "v_max3_f32 v12, v12, v51, v52\n\t" \
  "v_max3_f32 v12, v12, v53, v54\n\t" \
  "v_max3_f32 v12, v12, v55, v56\n\t" \
  "v_max3_f32 v12, v12, v57, v58\n\t" \
  "v_max3_f32 v12, v12, v59, v60\n\t" \
  "v_max3_f32 v12, v12, v61, v62\n\t" \
  "v_max_f32 v12, v12, v63\n\t" \
  "v_mov_b32 v13, v12\n\t" \
  "s_waitcnt lgkmcnt(0)\n\t" \
  "s_barrier\n\t" \
  "ds_read_b128 v[48:51], v15 offset:0\n\t" \
  "ds_read_b128 v[52:55], v15 offset:1024\n\t" \
  "ds_read_b128 v[56:59], v15 offset:2048\n\t" \
  "v_permlane32_swap_b32 v12, v13\n\t" \
  "v_mov_b64 v[80:81], 0\n\t" \
  "v_max_f32 v12, v12, v13\n\t" \
  "v_add_f32 v12, 0x41f00000, v12\n\t" \
  "s_mov_b32 exec_lo, 0\n\t" \
  "v_cvt_pk_bf16_f32 v104, -v12, 0\n\t" \
  "s_mov_b32 exec_lo, -1\n\t" \
  "v_mov_b64 v[82:83], 0\n\t" \
  "v_mov_b64 v[84:85], 0\n\t" \
  "v_mov_b64 v[86:87], 0\n\t" \
  "v_mov_b64 v[88:89], 0\n\t" \
  "v_mov_b64 v[90:91], 0\n\t" \
  "v_mfma_f32_32x32x8_bf16 v[32:47], v[116:117], v[104:105], v[32:47]\n\t" \
  "s_waitcnt lgkmcnt(0)\n\t" \
  "s_barrier\n\t" \
  "v_pk_add_f32 v[72:73], v[72:73], v[56:57]\n\t" \
  "v_pk_add_f32 v[64:65], v[64:65], v[48:49]\n\t" \
  "v_pk_add_f32 v[66:67], v[66:67], v[50:51]\n\t" \
  "v_pk_add_f32 v[68:69], v[68:69], v[52:53]\n\t" \
  "v_pk_add_f32 v[70:71], v[70:71], v[54:55]\n\t" \
  "v_pk_add_f32 v[74:75], v[74:75], v[58:59]\n\t" \
  "v_mov_b32 v10, v72\n\t" \
  "v_mov_b32 v11, v72\n\t" \
  "s_nop 1\n\t" \
  "v_permlane32_swap_b32 v10, v11\n\t" \
  "s_nop 0\n\t" \
  "v_rcp_f32 v10, v11\n\t" \
  "v_cmp_lt_f32 s[64:65], s66, v11\n\t" \
  "v_cmp_gt_f32 s[68:69], s67, v11\n\t" \
  "v_pk_mul_f32 v[64:65], v[64:65], v[10:11] op_sel_hi:[1,0]\n\t" \
  "v_pk_mul_f32 v[66:67], v[66:67], v[10:11] op_sel_hi:[1,0]\n\t" \
  "v_pk_mul_f32 v[68:69], v[68:69], v[10:11] op_sel_hi:[1,0]\n\t" \
  "v_pk_mul_f32 v[70:71], v[70:71], v[10:11] op_sel_hi:[1,0]\n\t" \
  "v_pk_mul_f32 v[72:73], v[72:73], v[10:11] op_sel_hi:[1,0]\n\t" \
  "v_pk_mul_f32 v[74:75], v[74:75], v[10:11] op_sel_hi:[1,0]\n\t" \
  "v_mul_f32 v12, v64, v65\n\t" \
  "v_fmac_f32 v12, v66, v67\n\t" \
  "v_fmac_f32 v12, v68, v69\n\t" \
  "v_fmac_f32 v12, v70, v71\n\t" \
  "v_fmac_f32 v12, v72, v73\n\t" \
  "v_fmac_f32 v12, v74, v75\n\t" \
  "s_and_b64 s[64:65], s[64:65], s[68:69]\n\t" \
  "v_cvt_pk_bf16_f32 v64, v64, v65\n\t" \
  "v_cvt_pk_bf16_f32 v65, v66, v67\n\t" \
  "v_cvt_pk_bf16_f32 v66, v68, v69\n\t" \
  "v_cmp_gt_f32 vcc, s67, abs(v12)\n\t" \
  "v_cvt_pk_bf16_f32 v67, v70, v71\n\t" \
  "v_cvt_pk_bf16_f32 v68, v72, v73\n\t" \
  "v_cvt_pk_bf16_f32 v69, v74, v75\n\t" \
  "s_and_b64 vcc, vcc, s[64:65]\n\t" \
  "s_cmp_eq_u64 vcc, -1\n\t" \
  "s_cbranch_scc1 2f\n\t" \
  "s_bitset1_b32 s63, 0\n\t" \
  "s_mov_b32 s53, 0x40000000\n\t" \
  "s_mov_b32 s54, 0x40000000\n\t" \
  "2:\n\t" \
  "buffer_store_dwordx4 v[64:67], v5, s[40:43], s54 offen sc1\n\t" \
  "buffer_store_dwordx2 v[68:69], v6, s[40:43], s54 offen sc1\n\t" \
  "v_exp_f32 v32, v32\n\t" \
  "v_exp_f32 v33, v33\n\t" \
  "s_mov_b64 exec, s[60:61]\n\t" \
  "ds_read_b128 v[24:27], v126 offset:8960\n\t" \
  "ds_read_b128 v[28:31], v126 offset:9600\n\t" \
  "s_mov_b64 exec, -1\n\t" \
  "v_exp_f32 v34, v34\n\t" \
  "v_exp_f32 v35, v35\n\t" \
  "v_exp_f32 v36, v36\n\t" \
  "v_exp_f32 v37, v37\n\t" \
  "v_mfma_f32_32x32x8_bf16 v[48:63], v[118:119], v[104:105], 0\n\t" \
  "s_setprio 2\n\t" \
  "v_exp_f32 v38, v38\n\t" \
  "v_exp_f32 v39, v39\n\t" \
  "v_exp_f32 v40, v40\n\t" \
  "v_exp_f32 v41, v41\n\t" \
  "v_mfma_f32_32x32x16_bf16 v[48:63], v[112:115], v[96:99], v[48:63]\n\t" \
  "ds_read_b128 v[108:111], v124 offset:17408\n\t" \
  "s_mov_b32 exec_hi, 0\n\t" \
  "ds_read_b64 v[116:117], v125 offset:33792\n\t" \
  "s_mov_b32 exec_hi, -1\n\t" \
  "v_exp_f32 v42, v42\n\t" \
  "v_exp_f32 v43, v43\n\t" \
  "v_exp_f32 v44, v44\n\t" \
  "v_exp_f32 v45, v45\n\t" \
  "v_smfmac_f32_32x32x32_bf16 v[80:95], v[16:19], v[32:39], v127\n\t" \
  "s_cmp_eq_u32 s44, 1\n\t" \
  "v_exp_f32 v46, v46\n\t" \
  "v_exp_f32 v47, v47\n\t" \
  "s_cbranch_scc1 4f\n\t" \
  "v_exp_f32 v48, v48\n\t" \
  "v_exp_f32 v49, v49\n\t" \
  "v_smfmac_f32_32x32x32_bf16 v[80:95], v[20:23], v[40:47], v127\n\t" \
  "s_waitcnt lgkmcnt(0)\n\t" \
  "s_mov_b64 exec, s[60:61]\n\t" \
  "ds_read_b128 v[16:19], v126 offset:7680\n\t" \
  "ds_read_b128 v[20:23], v126 offset:8320\n\t" \
  "s_mov_b64 exec, -1\n\t" \
  "v_exp_f32 v50, v50\n\t" \
  "v_exp_f32 v51, v51\n\t" \
  "v_exp_f32 v52, v52\n\t" \
  "v_exp_f32 v53, v53\n\t" \
  "v_mfma_f32_32x32x8_bf16 v[32:47], v[116:117], v[104:105], 0\n\t" \
  "s_waitcnt vmcnt(0)\n\t" \
  "v_mov_b64 v[10:11], 1\n\t" \
  "s_mov_b64 exec, 1\n\t" \
  "buffer_atomic_add_x2 v[10:11], off, s[40:43], s53\n\t" \
  "s_mov_b64 exec, -1\n\t" \
  "s_mov_b32 s53, 0x40000000\n\t" \
  "v_exp_f32 v54, v54\n\t" \
  "v_exp_f32 v55, v55\n\t" \
  "v_exp_f32 v56, v56\n\t" \
  "v_exp_f32 v57, v57\n\t" \
  "v_mfma_f32_32x32x16_bf16 v[32:47], v[108:111], v[96:99], v[32:47]\n\t" \
  "ds_read_b128 v[112:115], v124 offset:16896\n\t" \
  "s_mov_b32 exec_hi, 0\n\t" \
  "ds_read_b64 v[118:119], v125 offset:33280\n\t" \
  "s_mov_b32 exec_hi, -1\n\t" \
  "v_exp_f32 v58, v58\n\t" \
  "v_exp_f32 v59, v59\n\t" \
  "v_exp_f32 v60, v60\n\t" \
  "v_exp_f32 v61, v61\n\t" \
  "v_smfmac_f32_32x32x32_bf16 v[80:95], v[24:27], v[48:55], v127\n\t" \
  "s_cmp_eq_u32 s44, 2\n\t" \
  "v_exp_f32 v62, v62\n\t" \
  "v_exp_f32 v63, v63\n\t" \
  "s_cbranch_scc1 5f\n\t" \
  "v_exp_f32 v32, v32\n\t" \
  "v_exp_f32 v33, v33\n\t" \
  "v_smfmac_f32_32x32x32_bf16 v[80:95], v[28:31], v[56:63], v127\n\t" \
  "s_waitcnt lgkmcnt(0)\n\t" \
  "s_mov_b64 exec, s[60:61]\n\t" \
  "ds_read_b128 v[24:27], v126 offset:6400\n\t" \
  "ds_read_b128 v[28:31], v126 offset:7040\n\t" \
  "s_mov_b64 exec, -1\n\t" \
  "v_exp_f32 v34, v34\n\t" \
  "v_exp_f32 v35, v35\n\t" \
  "v_exp_f32 v36, v36\n\t" \
  "v_exp_f32 v37, v37\n\t" \
  "v_mfma_f32_32x32x8_bf16 v[48:63], v[118:119], v[104:105], 0\n\t" \
  "v_exp_f32 v38, v38\n\t" \
  "v_exp_f32 v39, v39\n\t" \
  "v_exp_f32 v40, v40\n\t" \
  "v_exp_f32 v41, v41\n\t" \
  "v_mfma_f32_32x32x16_bf16 v[48:63], v[112:115], v[96:99], v[48:63]\n\t" \
  "ds_read_b128 v[108:111], v124 offset:16384\n\t" \
  "s_mov_b32 exec_hi, 0\n\t" \
  "ds_read_b64 v[116:117], v125 offset:32768\n\t" \
  "s_mov_b32 exec_hi, -1\n\t" \
  "v_exp_f32 v42, v42\n\t" \
  "v_exp_f32 v43, v43\n\t" \
  "v_exp_f32 v44, v44\n\t" \
  "v_exp_f32 v45, v45\n\t" \
  "v_smfmac_f32_32x32x32_bf16 v[80:95], v[16:19], v[32:39], v127\n\t" \
  "s_cmp_eq_u32 s44, 3\n\t" \
  "v_xor_b32 v128, 0xff800000, v128\n\t" \
  "v_xor_b32 v129, 0xff800000, v129\n\t" \
  "v_xor_b32 v130, 0xff800000, v130\n\t" \
  "v_xor_b32 v131, 0xff800000, v131\n\t" \
  "v_exp_f32 v46, v46\n\t" \
  "v_exp_f32 v47, v47\n\t" \
  "s_cbranch_scc1 4f\n\t" \
  "v_exp_f32 v48, v48\n\t" \
  "v_exp_f32 v49, v49\n\t" \
  "v_smfmac_f32_32x32x32_bf16 v[80:95], v[20:23], v[40:47], v127\n\t" \
  "s_waitcnt lgkmcnt(0)\n\t" \
  "s_mov_b64 exec, s[60:61]\n\t" \
  "ds_read_b128 v[16:19], v126 offset:5120\n\t" \
  "ds_read_b128 v[20:23], v126 offset:5760\n\t" \
  "s_mov_b64 exec, -1\n\t" \
  "v_exp_f32 v50, v50\n\t" \
  "v_exp_f32 v51, v51\n\t" \
  "v_exp_f32 v52, v52\n\t" \
  "v_exp_f32 v53, v53\n\t" \
  "v_mfma_f32_32x32x8_bf16 v[32:47], v[116:117], v[104:105], 0\n\t" \
  "s_setprio 1\n\t" \
  "v_exp_f32 v54, v54\n\t" \
  "v_exp_f32 v55, v55\n\t" \
  "v_exp_f32 v56, v56\n\t" \
  "v_exp_f32 v57, v57\n\t" \
  "v_mfma_f32_32x32x16_bf16 v[32:47], v[108:111], v[96:99], v[32:47]\n\t" \
  "ds_read_b128 v[112:115], v124 offset:15872\n\t" \
  "s_mov_b32 exec_hi, 0\n\t" \
  "ds_read_b64 v[118:119], v125 offset:32256\n\t" \
  "s_mov_b32 exec_hi, -1\n\t" \
  "v_exp_f32 v58, v58\n\t" \
  "v_exp_f32 v59, v59\n\t" \
  "v_exp_f32 v60, v60\n\t" \
  "v_exp_f32 v61, v61\n\t" \
  "v_smfmac_f32_32x32x32_bf16 v[80:95], v[24:27], v[48:55], v127\n\t" \
  "s_cmp_eq_u32 s44, 4\n\t" \
  "v_xor_b32 v132, 0xff800000, v132\n\t" \
  "v_xor_b32 v133, 0xff800000, v133\n\t" \
  "v_xor_b32 v134, 0xff800000, v134\n\t" \
  "v_xor_b32 v135, 0xff800000, v135\n\t" \
  "v_exp_f32 v62, v62\n\t" \
  "v_exp_f32 v63, v63\n\t" \
  "s_cbranch_scc1 5f\n\t" \
  "v_exp_f32 v32, v32\n\t" \
  "v_exp_f32 v33, v33\n\t" \
  "v_smfmac_f32_32x32x32_bf16 v[80:95], v[28:31], v[56:63], v127\n\t" \
  "s_waitcnt lgkmcnt(0)\n\t" \
  "s_mov_b64 exec, s[60:61]\n\t" \
  "ds_read_b128 v[24:27], v126 offset:3840\n\t" \
  "ds_read_b128 v[28:31], v126 offset:4480\n\t" \
  "s_mov_b64 exec, -1\n\t" \
  "v_exp_f32 v34, v34\n\t" \
  "v_exp_f32 v35, v35\n\t" \
  "v_exp_f32 v36, v36\n\t" \
  "v_exp_f32 v37, v37\n\t" \
  "v_mfma_f32_32x32x8_bf16 v[48:63], v[118:119], v[104:105], 0\n\t" \
  "v_exp_f32 v38, v38\n\t" \
  "v_exp_f32 v39, v39\n\t" \
  "v_exp_f32 v40, v40\n\t" \
  "v_exp_f32 v41, v41\n\t" \
  "v_mfma_f32_32x32x16_bf16 v[48:63], v[112:115], v[96:99], v[48:63]\n\t" \
  "ds_read_b128 v[108:111], v124 offset:15360\n\t" \
  "s_mov_b32 exec_hi, 0\n\t" \
  "ds_read_b64 v[116:117], v125 offset:31744\n\t" \
  "s_mov_b32 exec_hi, -1\n\t" \
  "v_exp_f32 v42, v42\n\t" \
  "v_exp_f32 v43, v43\n\t" \
  "v_exp_f32 v44, v44\n\t" \
  "v_exp_f32 v45, v45\n\t" \
  "v_smfmac_f32_32x32x32_bf16 v[80:95], v[16:19], v[32:39], v127\n\t" \
  "s_cmp_eq_u32 s44, 5\n\t" \
  "v_xor_b32 v136, 0xff800000, v136\n\t" \
  "v_xor_b32 v137, 0xff800000, v137\n\t" \
  "v_xor_b32 v138, 0xff800000, v138\n\t" \
  "v_xor_b32 v139, 0xff800000, v139\n\t" \
  "v_exp_f32 v46, v46\n\t" \
  "v_exp_f32 v47, v47\n\t" \
  "s_cbranch_scc1 4f\n\t" \
  "v_exp_f32 v48, v48\n\t" \
  "v_exp_f32 v49, v49\n\t" \
  "v_smfmac_f32_32x32x32_bf16 v[80:95], v[20:23], v[40:47], v127\n\t" \
  "s_waitcnt lgkmcnt(0)\n\t" \
  "s_mov_b64 exec, s[60:61]\n\t" \
  "ds_read_b128 v[16:19], v126 offset:2560\n\t" \
  "ds_read_b128 v[20:23], v126 offset:3200\n\t" \
  "s_mov_b64 exec, -1\n\t" \
  "v_exp_f32 v50, v50\n\t" \
  "v_exp_f32 v51, v51\n\t" \
  "v_exp_f32 v52, v52\n\t" \
  "v_exp_f32 v53, v53\n\t" \
  "v_mfma_f32_32x32x8_bf16 v[32:47], v[116:117], v[104:105], 0\n\t" \
  "v_exp_f32 v54, v54\n\t" \
  "v_exp_f32 v55, v55\n\t" \
  "v_exp_f32 v56, v56\n\t" \
  "v_exp_f32 v57, v57\n\t" \
  "v_mfma_f32_32x32x16_bf16 v[32:47], v[108:111], v[96:99], v[32:47]\n\t" \
  "ds_read_b128 v[112:115], v124 offset:14848\n\t" \
  "s_mov_b32 exec_hi, 0\n\t" \
  "ds_read_b64 v[118:119], v125 offset:31232\n\t" \
  "s_mov_b32 exec_hi, -1\n\t" \
  "v_exp_f32 v58, v58\n\t" \
  "v_exp_f32 v59, v59\n\t" \
  "v_exp_f32 v60, v60\n\t" \
  "v_exp_f32 v61, v61\n\t" \
  "v_smfmac_f32_32x32x32_bf16 v[80:95], v[24:27], v[48:55], v127\n\t" \
  "s_cmp_eq_u32 s44, 6\n\t" \
  "v_xor_b32 v140, 0xff800000, v140\n\t" \
  "v_xor_b32 v141, 0xff800000, v141\n\t" \
  "v_xor_b32 v142, 0xff800000, v142\n\t" \
  "v_xor_b32 v143, 0xff800000, v143\n\t" \
  "v_exp_f32 v62, v62\n\t" \
  "v_exp_f32 v63, v63\n\t" \
  "s_cbranch_scc1 5f\n\t" \
  "v_exp_f32 v32, v32\n\t" \
  "v_exp_f32 v33, v33\n\t" \
  "v_smfmac_f32_32x32x32_bf16 v[80:95], v[28:31], v[56:63], v127\n\t" \
  "s_waitcnt lgkmcnt(0)\n\t" \
  "s_mov_b64 exec, s[60:61]\n\t" \
  "ds_read_b128 v[24:27], v126 offset:1280\n\t" \
  "ds_read_b128 v[28:31], v126 offset:1920\n\t" \
  "s_mov_b64 exec, -1\n\t" \
  "v_exp_f32 v34, v34\n\t" \
  "v_exp_f32 v35, v35\n\t" \
  "v_exp_f32 v36, v36\n\t" \
  "v_exp_f32 v37, v37\n\t" \
  "v_mfma_f32_32x32x8_bf16 v[48:63], v[118:119], v[104:105], 0\n\t" \
  "s_setprio 0\n\t" \
  "v_exp_f32 v38, v38\n\t" \
  "v_exp_f32 v39, v39\n\t" \
  "v_exp_f32 v40, v40\n\t" \
  "v_exp_f32 v41, v41\n\t" \
  "v_mfma_f32_32x32x16_bf16 v[48:63], v[112:115], v[96:99], v[48:63]\n\t" \
  "ds_read_b128 v[108:111], v124 offset:14336\n\t" \
  "s_mov_b32 exec_hi, 0\n\t" \
  "ds_read_b64 v[116:117], v125 offset:30720\n\t" \
  "s_mov_b32 exec_hi, -1\n\t" \
  "v_exp_f32 v42, v42\n\t" \
  "v_exp_f32 v43, v43\n\t" \
  "v_exp_f32 v44, v44\n\t" \
  "v_exp_f32 v45, v45\n\t" \
  "v_smfmac_f32_32x32x32_bf16 v[80:95], v[16:19], v[32:39], v127\n\t" \
  "s_cmp_eq_u32 s44, 7\n\t" \
  "v_exp_f32 v46, v46\n\t" \
  "v_exp_f32 v47, v47\n\t" \
  "s_cbranch_scc1 4f\n\t" \
  "v_exp_f32 v48, v48\n\t" \
  "v_exp_f32 v49, v49\n\t" \
  "v_smfmac_f32_32x32x32_bf16 v[80:95], v[20:23], v[40:47], v127\n\t" \
  "s_waitcnt lgkmcnt(0)\n\t" \
  "s_mov_b64 exec, s[60:61]\n\t" \
  "ds_read_b128 v[16:19], v126 offset:0\n\t" \
  "ds_read_b128 v[20:23], v126 offset:640\n\t" \
  "s_mov_b64 exec, -1\n\t" \
  "v_exp_f32 v50, v50\n\t" \
  "v_exp_f32 v51, v51\n\t" \
  "v_exp_f32 v52, v52\n\t" \
  "v_exp_f32 v53, v53\n\t" \
  "v_mfma_f32_32x32x8_bf16 v[32:47], v[116:117], v[104:105], v[128:143]\n\t" \
  "v_exp_f32 v54, v54\n\t" \
  "v_exp_f32 v55, v55\n\t" \
  "v_exp_f32 v56, v56\n\t" \
  "v_exp_f32 v57, v57\n\t" \
  "v_mfma_f32_32x32x16_bf16 v[32:47], v[108:111], v[96:99], v[32:47]\n\t" \
  "v_exp_f32 v58, v58\n\t" \
  "v_exp_f32 v59, v59\n\t" \
  "v_exp_f32 v60, v60\n\t" \
  "v_exp_f32 v61, v61\n\t" \
  "v_smfmac_f32_32x32x32_bf16 v[80:95], v[24:27], v[48:55], v127\n\t" \
  "s_cmp_eq_u32 s44, 8\n\t" \
  "v_exp_f32 v62, v62\n\t" \
  "v_exp_f32 v63, v63\n\t" \
  "s_cbranch_scc1 5f\n\t" \
  "v_exp_f32 v32, v32\n\t" \
  "v_exp_f32 v33, v33\n\t" \
  "v_smfmac_f32_32x32x32_bf16 v[80:95], v[28:31], v[56:63], v127\n\t" \
  "s_waitcnt lgkmcnt(0)\n\t" \
  "v_exp_f32 v34, v34\n\t" \
  "v_exp_f32 v35, v35\n\t" \
  "v_exp_f32 v36, v36\n\t" \
  "v_exp_f32 v37, v37\n\t" \
  "v_exp_f32 v38, v38\n\t" \
  "v_exp_f32 v39, v39\n\t" \
  "v_exp_f32 v40, v40\n\t" \
  "v_exp_f32 v41, v41\n\t" \
  "v_exp_f32 v42, v42\n\t" \
  "v_exp_f32 v43, v43\n\t" \
  "v_exp_f32 v44, v44\n\t" \
  "v_exp_f32 v45, v45\n\t" \
  "v_smfmac_f32_32x32x32_bf16 v[80:95], v[16:19], v[32:39], v127\n\t" \
  "v_exp_f32 v46, v46\n\t" \
  "v_exp_f32 v47, v47\n\t" \
  "s_nop 1\n\t" \
  "v_smfmac_f32_32x32x32_bf16 v[80:95], v[20:23], v[40:47], v127\n\t" \
  "s_branch 6f\n\t" \
  "4:\n\t" \
  "s_nop 0\n\t" \
  "v_smfmac_f32_32x32x32_bf16 v[80:95], v[20:23], v[40:47], v127\n\t" \
  "s_branch 9f\n\t" \
  "5:\n\t" \
  "s_nop 0\n\t" \
  "v_smfmac_f32_32x32x32_bf16 v[80:95], v[28:31], v[56:63], v127\n\t" \
  "9:\n\t" \
  "s_waitcnt vmcnt(0)\n\t" \
  "v_mov_b64 v[10:11], 1\n\t" \
  "s_mov_b64 exec, 1\n\t" \
  "buffer_atomic_add_x2 v[10:11], off, s[40:43], s53\n\t" \
  "s_mov_b64 exec, -1\n\t" \
  "s_mov_b32 s53, 0x40000000\n\t" \
  "6:\n\t" \
  "s_setprio 0\n\t" \
  "s_waitcnt lgkmcnt(0)\n\t" \
  "s_nop 8\n\t" \
  "v_mov_b32 v10, v88\n\t" \
  "v_mov_b32 v11, v88\n\t" \
  "s_nop 1\n\t" \
  "v_permlane32_swap_b32 v10, v11\n\t" \
  "s_nop 0\n\t" \
  "v_rcp_f32 v10, v11\n\t" \
  "v_cmp_lt_f32 s[64:65], s66, v11\n\t" \
  "v_cmp_gt_f32 s[68:69], s67, v11\n\t" \
  "v_pk_mul_f32 v[80:81], v[80:81], v[10:11] op_sel_hi:[1,0]\n\t" \
  "v_pk_mul_f32 v[82:83], v[82:83], v[10:11] op_sel_hi:[1,0]\n\t" \
  "v_pk_mul_f32 v[84:85], v[84:85], v[10:11] op_sel_hi:[1,0]\n\t" \
  "v_pk_mul_f32 v[86:87], v[86:87], v[10:11] op_sel_hi:[1,0]\n\t" \
  "v_pk_mul_f32 v[88:89], v[88:89], v[10:11] op_sel_hi:[1,0]\n\t" \
  "v_pk_mul_f32 v[90:91], v[90:91], v[10:11] op_sel_hi:[1,0]\n\t" \
  "v_mul_f32 v12, v80, v81\n\t" \
  "v_fmac_f32 v12, v82, v83\n\t" \
  "v_fmac_f32 v12, v84, v85\n\t" \
  "v_fmac_f32 v12, v86, v87\n\t" \
  "v_fmac_f32 v12, v88, v89\n\t" \
  "v_fmac_f32 v12, v90, v91\n\t" \
  "s_and_b64 s[64:65], s[64:65], s[68:69]\n\t" \
  "v_cvt_pk_bf16_f32 v80, v80, v81\n\t" \
  "v_cvt_pk_bf16_f32 v81, v82, v83\n\t" \
  "v_cvt_pk_bf16_f32 v82, v84, v85\n\t" \
  "v_cmp_gt_f32 vcc, s67, abs(v12)\n\t" \
  "v_cvt_pk_bf16_f32 v83, v86, v87\n\t" \
  "v_cvt_pk_bf16_f32 v84, v88, v89\n\t" \
  "v_cvt_pk_bf16_f32 v85, v90, v91\n\t" \
  "s_and_b64 vcc, vcc, s[64:65]\n\t" \
  "s_cmp_eq_u64 vcc, -1\n\t" \
  "s_cbranch_scc1 2f\n\t" \
  "s_bitset1_b32 s63, 1\n\t" \
  "s_mov_b32 s56, 0x40000000\n\t" \
  "2:\n\t" \
  "buffer_store_dwordx4 v[80:83], v5, s[40:43], s56 offen sc1\n\t" \
  "buffer_store_dwordx2 v[84:85], v6, s[40:43], s56 offen sc1\n\t" \
  "s_bitcmp1_b32 s63, 0\n\t" \
  "s_cbranch_scc1 3f\n\t" \
  "s_barrier\n\t" \
  "3:\n\t" \
  "s_nop 1\n\t" \
  ""

#define TAIL_ASM \
  "s_cmp_eq_u32 s92, 2\n\t" \
  "s_cbranch_scc0 7f\n\t" \
  "s_mov_b32 s50, 0x100000\n\t" \
  "s_branch 0f\n\t" \
  "1:\n\t" \
  "s_sleep 2\n\t" \
  "0:\n\t" \
  "v_mov_b64 v[108:109], v[8:9]\n\t" \
  "s_mov_b64 exec, s[88:89]\n\t" \
  "v_mov_b64 v[108:109], 0\n\t" \
  "buffer_atomic_add_x2 v[108:109], v163, s[40:43], s93 offen sc0\n\t" \
  "s_mov_b64 exec, -1\n\t" \
  "10:\n\t" \
  "s_sub_u32 s50, s50, 1\n\t" \
  "s_waitcnt vmcnt(0)\n\t" \
  "v_cmp_ne_u64 vcc, v[8:9], v[108:109]\n\t" \
  "s_cbranch_scc1 3f\n\t" \
  "s_cbranch_vccnz 1b\n\t" \
  "3:\n\t" \
  "buffer_load_dwordx4 v[32:35], v160, s[40:43], s90 offen offset:0 sc1\n\t" \
  "buffer_load_dwordx4 v[36:39], v160, s[40:43], s90 offen offset:256 sc1\n\t" \
  "s_add_u32 s90, s90, 0x20000\n\t" \
  "buffer_load_dwordx4 v[40:43], v160, s[40:43], s90 offen offset:0 sc1\n\t" \
  "buffer_load_dwordx4 v[44:47], v160, s[40:43], s90 offen offset:256 sc1\n\t" \
  "s_add_u32 s90, s90, 0x20000\n\t" \
  "buffer_load_dwordx4 v[48:51], v160, s[40:43], s90 offen offset:0 sc1\n\t" \
  "buffer_load_dwordx4 v[52:55], v160, s[40:43], s90 offen offset:256 sc1\n\t" \
  "s_add_u32 s90, s90, 0x20000\n\t" \
  "buffer_load_dwordx4 v[56:59], v160, s[40:43], s90 offen offset:0 sc1\n\t" \
  "buffer_load_dwordx4 v[60:63], v160, s[40:43], s90 offen offset:256 sc1\n\t" \
  "s_add_u32 s90, s90, 0x20000\n\t" \
  "buffer_load_dwordx4 v[64:67], v160, s[40:43], s90 offen offset:0 sc1\n\t" \
  "buffer_load_dwordx4 v[68:71], v160, s[40:43], s90 offen offset:256 sc1\n\t" \
  "s_add_u32 s90, s90, 0x20000\n\t" \
  "buffer_load_dwordx4 v[72:75], v160, s[40:43], s90 offen offset:0 sc1\n\t" \
  "buffer_load_dwordx4 v[76:79], v160, s[40:43], s90 offen offset:256 sc1\n\t" \
  "s_add_u32 s90, s90, 0x20000\n\t" \
  "s_barrier\n\t" \
  "ds_write_b128 v12, v[112:115] offset:0\n\t" \
  "ds_write_b128 v12, v[116:119] offset:4096\n\t" \
  "ds_write_b128 v12, v[120:123] offset:8192\n\t" \
  "ds_write_b128 v12, v[124:127] offset:12288\n\t" \
  "ds_write_b128 v12, v[128:131] offset:16384\n\t" \
  "ds_write_b128 v12, v[132:135] offset:20480\n\t" \
  "ds_write_b128 v12, v[136:139] offset:24576\n\t" \
  "ds_write_b128 v12, v[140:143] offset:28672\n\t" \
  "ds_write_b128 v12, v[144:147] offset:32768\n\t" \
  "ds_write_b128 v12, v[148:151] offset:36864\n\t" \
  "ds_write_b128 v12, v[152:155] offset:40960\n\t" \
  "s_waitcnt lgkmcnt(0)\n\t" \
  "s_barrier\n\t" \
  "buffer_load_dwordx4 v[80:83], v160, s[40:43], s90 offen offset:0 sc1\n\t" \
  "buffer_load_dwordx4 v[84:87], v160, s[40:43], s90 offen offset:256 sc1\n\t" \
  "s_add_u32 s90, s90, 0x20000\n\t" \
  "buffer_load_dwordx4 v[88:91], v160, s[40:43], s90 offen offset:0 sc1\n\t" \
  "buffer_load_dwordx4 v[92:95], v160, s[40:43], s90 offen offset:256 sc1\n\t" \
  "s_add_u32 s90, s90, 0x20000\n\t" \
  "buffer_load_dwordx4 v[96:99], v160, s[40:43], s90 offen offset:0 sc1\n\t" \
  "buffer_load_dwordx4 v[100:103], v160, s[40:43], s90 offen offset:256 sc1\n\t" \
  "s_add_u32 s90, s90, 0x20000\n\t" \
  "buffer_load_dwordx4 v[104:107], v160, s[40:43], s90 offen offset:0 sc1\n\t" \
  "buffer_load_dwordx4 v[108:111], v160, s[40:43], s90 offen offset:256 sc1\n\t" \
  "s_add_u32 s90, s90, 0x20000\n\t" \
  "s_setprio 0\n\t" \
  "v_xor_b32 v166, 32, v161\n\t" \
  "v_xor_b32 v11, 64, v161\n\t" \
  "v_xor_b32 v15, 96, v161\n\t" \
  "v_xor_b32 v10, 64, v162\n\t" \
  "v_xor_b32 v14, 16, v164\n\t" \
  "ds_read_b64_tr_b16 v[144:145], v161 offset:0\n\t" \
  "ds_read_b64_tr_b16 v[146:147], v161 offset:512\n\t" \
  "ds_read_b64_tr_b16 v[148:149], v166 offset:0\n\t" \
  "ds_read_b64_tr_b16 v[150:151], v166 offset:512\n\t" \
  "ds_read_b64_tr_b16 v[152:153], v11 offset:0\n\t" \
  "ds_read_b64_tr_b16 v[154:155], v11 offset:512\n\t" \
  "ds_read_b64_tr_b16 v[156:157], v15 offset:0\n\t" \
  "ds_read_b64_tr_b16 v[158:159], v15 offset:512\n\t" \
  "ds_read_b64_tr_b16 v[16:17], v161 offset:4096\n\t" \
  "ds_read_b64_tr_b16 v[18:19], v161 offset:4608\n\t" \
  "ds_read_b64_tr_b16 v[20:21], v166 offset:4096\n\t" \
  "ds_read_b64_tr_b16 v[22:23], v166 offset:4608\n\t" \
  "ds_read_b64_tr_b16 v[24:25], v11 offset:4096\n\t" \
  "ds_read_b64_tr_b16 v[26:27], v11 offset:4608\n\t" \
  "ds_read_b64_tr_b16 v[28:29], v15 offset:4096\n\t" \
  "ds_read_b64_tr_b16 v[30:31], v15 offset:4608\n\t" \
  "s_waitcnt vmcnt(18) lgkmcnt(12)\n\t" \
  "v_mfma_f32_16x16x32_bf16 v[112:115], v[144:147], v[32:35], 0\n\t" \
  "ds_read_b64_tr_b16 v[0:1], v161 offset:8192\n\t" \
  "ds_read_b64_tr_b16 v[2:3], v161 offset:8704\n\t" \
  "v_mfma_f32_16x16x32_bf16 v[116:119], v[144:147], v[36:39], 0\n\t" \
  "ds_read_b64_tr_b16 v[4:5], v166 offset:8192\n\t" \
  "v_mfma_f32_16x16x32_bf16 v[124:127], v[148:151], v[36:39], 0\n\t" \
  "ds_read_b64_tr_b16 v[6:7], v166 offset:8704\n\t" \
  "v_mfma_f32_16x16x32_bf16 v[120:123], v[148:151], v[32:35], 0\n\t" \
  "s_waitcnt lgkmcnt(12)\n\t" \
  "v_mfma_f32_16x16x32_bf16 v[128:131], v[152:155], v[32:35], 0\n\t" \
  "ds_read_b64_tr_b16 v[144:145], v11 offset:8192\n\t" \
  "ds_read_b64_tr_b16 v[146:147], v11 offset:8704\n\t" \
  "v_mfma_f32_16x16x32_bf16 v[132:135], v[152:155], v[36:39], 0\n\t" \
  "ds_read_b64_tr_b16 v[148:149], v15 offset:8192\n\t" \
  "v_mfma_f32_16x16x32_bf16 v[140:143], v[156:159], v[36:39], 0\n\t" \
  "ds_read_b64_tr_b16 v[150:151], v15 offset:8704\n\t" \
  "v_mfma_f32_16x16x32_bf16 v[136:139], v[156:159], v[32:35], 0\n\t" \
  "s_waitcnt vmcnt(16) lgkmcnt(12)\n\t" \
  "v_mfma_f32_16x16x32_bf16 v[112:115], v[16:19], v[40:43], v[112:115]\n\t" \
  "ds_read_b64_tr_b16 v[152:153], v161 offset:12288\n\t" \
  "ds_read_b64_tr_b16 v[154:155], v161 offset:12800\n\t" \
  "v_mfma_f32_16x16x32_bf16 v[116:119], v[16:19], v[44:47], v[116:119]\n\t" \
  "ds_read_b64_tr_b16 v[156:157], v166 offset:12288\n\t" \
  "v_mfma_f32_16x16x32_bf16 v[124:127], v[20:23], v[44:47], v[124:127]\n\t" \
  "ds_read_b64_tr_b16 v[158:159], v166 offset:12800\n\t" \
  "v_mfma_f32_16x16x32_bf16 v[120:123], v[20:23], v[40:43], v[120:123]\n\t" \
  "s_waitcnt lgkmcnt(12)\n\t" \
  "v_mfma_f32_16x16x32_bf16 v[128:131], v[24:27], v[40:43], v[128:131]\n\t" \
  "ds_read_b64_tr_b16 v[16:17], v11 offset:12288\n\t" \
  "ds_read_b64_tr_b16 v[18:19], v11 offset:12800\n\t" \
  "v_mfma_f32_16x16x32_bf16 v[132:135], v[24:27], v[44:47], v[132:135]\n\t" \
  "ds_read_b64_tr_b16 v[20:21], v15 offset:12288\n\t" \
  "v_mfma_f32_16x16x32_bf16 v[140:143], v[28:31], v[44:47], v[140:143]\n\t" \
  "ds_read_b64_tr_b16 v[22:23], v15 offset:12800\n\t" \
  "v_mfma_f32_16x16x32_bf16 v[136:139], v[28:31], v[40:43], v[136:139]\n\t" \
  "s_waitcnt vmcnt(14) lgkmcnt(12)\n\t" \
  "v_mfma_f32_16x16x32_bf16 v[112:115], v[0:3], v[48:51], v[112:115]\n\t" \
  "ds_read_b64_tr_b16 v[24:25], v161 offset:16384\n\t" \
  "ds_read_b64_tr_b16 v[26:27], v161 offset:16896\n\t" \
  "v_mfma_f32_16x16x32_bf16 v[116:119], v[0:3], v[52:55], v[116:119]\n\t" \
  "ds_read_b64_tr_b16 v[28:29], v166 offset:16384\n\t" \
  "v_mfma_f32_16x16x32_bf16 v[124:127], v[4:7], v[52:55], v[124:127]\n\t" \
  "ds_read_b64_tr_b16 v[30:31], v166 offset:16896\n\t" \
  "v_mfma_f32_16x16x32_bf16 v[120:123], v[4:7], v[48:51], v[120:123]\n\t" \
  "s_waitcnt lgkmcnt(12)\n\t" \
  "v_mfma_f32_16x16x32_bf16 v[128:131], v[144:147], v[48:51], v[128:131]\n\t" \
  "ds_read_b64_tr_b16 v[0:1], v11 offset:16384\n\t" \
  "ds_read_b64_tr_b16 v[2:3], v11 offset:16896\n\t" \
  "v_mfma_f32_16x16x32_bf16 v[132:135], v[144:147], v[52:55], v[132:135]\n\t" \
  "ds_read_b64_tr_b16 v[4:5], v15 offset:16384\n\t" \
  "v_mfma_f32_16x16x32_bf16 v[140:143], v[148:151], v[52:55], v[140:143]\n\t" \
  "ds_read_b64_tr_b16 v[6:7], v15 offset:16896\n\t" \
  "v_mfma_f32_16x16x32_bf16 v[136:139], v[148:151], v[48:51], v[136:139]\n\t" \
  "s_waitcnt vmcnt(12) lgkmcnt(12)\n\t" \
  "v_mfma_f32_16x16x32_bf16 v[112:115], v[152:155], v[56:59], v[112:115]\n\t" \
  "ds_read_b64_tr_b16 v[144:145], v161 offset:20480\n\t" \
  "ds_read_b64_tr_b16 v[146:147], v161 offset:20992\n\t" \
  "v_mfma_f32_16x16x32_bf16 v[116:119], v[152:155], v[60:63], v[116:119]\n\t" \
  "ds_read_b64_tr_b16 v[148:149], v166 offset:20480\n\t" \
  "v_mfma_f32_16x16x32_bf16 v[124:127], v[156:159], v[60:63], v[124:127]\n\t" \
  "ds_read_b64_tr_b16 v[150:151], v166 offset:20992\n\t" \
  "v_mfma_f32_16x16x32_bf16 v[120:123], v[156:159], v[56:59], v[120:123]\n\t" \
  "s_waitcnt lgkmcnt(12)\n\t" \
  "v_mfma_f32_16x16x32_bf16 v[128:131], v[16:19], v[56:59], v[128:131]\n\t" \
  "ds_read_b64_tr_b16 v[152:153], v11 offset:20480\n\t" \
  "ds_read_b64_tr_b16 v[154:155], v11 offset:20992\n\t" \
  "v_mfma_f32_16x16x32_bf16 v[132:135], v[16:19], v[60:63], v[132:135]\n\t" \
  "ds_read_b64_tr_b16 v[156:157], v15 offset:20480\n\t" \
  "v_mfma_f32_16x16x32_bf16 v[140:143], v[20:23], v[60:63], v[140:143]\n\t" \
  "ds_read_b64_tr_b16 v[158:159], v15 offset:20992\n\t" \
  "v_mfma_f32_16x16x32_bf16 v[136:139], v[20:23], v[56:59], v[136:139]\n\t" \
  "s_waitcnt vmcnt(10) lgkmcnt(12)\n\t" \
  "v_mfma_f32_16x16x32_bf16 v[112:115], v[24:27], v[64:67], v[112:115]\n\t" \
  "ds_read_b64_tr_b16 v[16:17], v161 offset:24576\n\t" \
  "ds_read_b64_tr_b16 v[18:19], v161 offset:25088\n\t" \
  "v_mfma_f32_16x16x32_bf16 v[116:119], v[24:27], v[68:71], v[116:119]\n\t" \
  "ds_read_b64_tr_b16 v[20:21], v166 offset:24576\n\t" \
  "v_mfma_f32_16x16x32_bf16 v[124:127], v[28:31], v[68:71], v[124:127]\n\t" \
  "ds_read_b64_tr_b16 v[22:23], v166 offset:25088\n\t" \
  "v_mfma_f32_16x16x32_bf16 v[120:123], v[28:31], v[64:67], v[120:123]\n\t" \
  "s_waitcnt lgkmcnt(12)\n\t" \
  "v_mfma_f32_16x16x32_bf16 v[128:131], v[0:3], v[64:67], v[128:131]\n\t" \
  "ds_read_b64_tr_b16 v[24:25], v11 offset:24576\n\t" \
  "ds_read_b64_tr_b16 v[26:27], v11 offset:25088\n\t" \
  "v_mfma_f32_16x16x32_bf16 v[132:135], v[0:3], v[68:71], v[132:135]\n\t" \
  "ds_read_b64_tr_b16 v[28:29], v15 offset:24576\n\t" \
  "v_mfma_f32_16x16x32_bf16 v[140:143], v[4:7], v[68:71], v[140:143]\n\t" \
  "ds_read_b64_tr_b16 v[30:31], v15 offset:25088\n\t" \
  "v_mfma_f32_16x16x32_bf16 v[136:139], v[4:7], v[64:67], v[136:139]\n\t" \
  "s_branch 3f\n\t" \
  "7:\n\t" \
  "s_cmp_eq_u32 s92, 3\n\t" \
  "s_cbranch_scc0 4f\n\t" \
  "buffer_load_dwordx4 v[32:35], v160, s[40:43], s90 offen offset:0 sc1\n\t" \
  "s_add_u32 s90, s90, 0x20000\n\t" \
  "buffer_load_dwordx4 v[36:39], v160, s[40:43], s90 offen offset:0 sc1\n\t" \
  "s_add_u32 s90, s90, 0x20000\n\t" \
  "buffer_load_dwordx4 v[40:43], v160, s[40:43], s90 offen offset:0 sc1\n\t" \
  "s_add_u32 s90, s90, 0x20000\n\t" \
  "buffer_load_dwordx4 v[44:47], v160, s[40:43], s90 offen offset:0 sc1\n\t" \
  "s_add_u32 s90, s90, 0x20000\n\t" \
  "buffer_load_dwordx4 v[48:51], v160, s[40:43], s90 offen offset:0 sc1\n\t" \
  "s_add_u32 s90, s90, 0x20000\n\t" \
  "buffer_load_dwordx4 v[52:55], v160, s[40:43], s90 offen offset:0 sc1\n\t" \
  "s_add_u32 s90, s90, 0x20000\n\t" \
  "buffer_load_dwordx4 v[56:59], v160, s[40:43], s90 offen offset:0 sc1\n\t" \
  "s_add_u32 s90, s90, 0x20000\n\t" \
  "buffer_load_dwordx4 v[60:63], v160, s[40:43], s90 offen offset:0 sc1\n\t" \
  "s_add_u32 s90, s90, 0x20000\n\t" \
  "buffer_load_dwordx4 v[64:67], v160, s[40:43], s90 offen offset:0 sc1\n\t" \
  "s_add_u32 s90, s90, 0x20000\n\t" \
  "buffer_load_dwordx4 v[68:71], v160, s[40:43], s90 offen offset:0 sc1\n\t" \
  "s_add_u32 s90, s90, 0x20000\n\t" \
  "s_barrier\n\t" \
  "s_waitcnt vmcnt(10)\n\t" \
  "v_mov_b64 v[10:11], 1\n\t" \
  "s_mov_b64 exec, 1\n\t" \
  "buffer_atomic_add_x2 v[10:11], off, s[40:43], s91\n\t" \
  "s_mov_b64 exec, -1\n\t" \
  "s_mov_b32 s91, 0x40000000\n\t" \
  "v_xor_b32 v166, 32, v161\n\t" \
  "v_xor_b32 v11, 64, v161\n\t" \
  "v_xor_b32 v15, 96, v161\n\t" \
  "v_xor_b32 v10, 64, v162\n\t" \
  "v_xor_b32 v14, 16, v164\n\t" \
  "ds_read_b64_tr_b16 v[144:145], v161 offset:0\n\t" \
  "ds_read_b64_tr_b16 v[146:147], v161 offset:512\n\t" \
  "ds_read_b64_tr_b16 v[148:149], v166 offset:0\n\t" \
  "ds_read_b64_tr_b16 v[150:151], v166 offset:512\n\t" \
  "ds_read_b64_tr_b16 v[152:153], v11 offset:0\n\t" \
  "ds_read_b64_tr_b16 v[154:155], v11 offset:512\n\t" \
  "ds_read_b64_tr_b16 v[156:157], v15 offset:0\n\t" \
  "ds_read_b64_tr_b16 v[158:159], v15 offset:512\n\t" \
  "ds_read_b64_tr_b16 v[16:17], v161 offset:4096\n\t" \
  "ds_read_b64_tr_b16 v[18:19], v161 offset:4608\n\t" \
  "ds_read_b64_tr_b16 v[20:21], v166 offset:4096\n\t" \
  "ds_read_b64_tr_b16 v[22:23], v166 offset:4608\n\t" \
  "ds_read_b64_tr_b16 v[24:25], v11 offset:4096\n\t" \
  "ds_read_b64_tr_b16 v[26:27], v11 offset:4608\n\t" \
  "ds_read_b64_tr_b16 v[28:29], v15 offset:4096\n\t" \
  "ds_read_b64_tr_b16 v[30:31], v15 offset:4608\n\t" \
  "s_branch 6f\n\t" \
  "4:\n\t" \
  "s_waitcnt vmcnt(0)\n\t" \
  "v_mov_b64 v[10:11], 1\n\t" \
  "s_mov_b64 exec, 1\n\t" \
  "buffer_atomic_add_x2 v[10:11], off, s[40:43], s91\n\t" \
  "s_mov_b64 exec, -1\n\t" \
  "s_mov_b32 s91, 0x40000000\n\t" \
  "s_mov_b32 s50, 0x100000\n\t" \
  "v_mov_b64 v[108:109], v[8:9]\n\t" \
  "s_mov_b64 exec, s[88:89]\n\t" \
  "v_mov_b64 v[108:109], 0\n\t" \
  "buffer_atomic_add_x2 v[108:109], v163, s[40:43], s93 offen sc0\n\t" \
  "s_mov_b64 exec, -1\n\t" \
  "s_barrier\n\t" \
  "v_xor_b32 v166, 32, v161\n\t" \
  "v_xor_b32 v11, 64, v161\n\t" \
  "v_xor_b32 v15, 96, v161\n\t" \
  "v_xor_b32 v10, 64, v162\n\t" \
  "v_xor_b32 v14, 16, v164\n\t" \
  "ds_read_b64_tr_b16 v[144:145], v161 offset:0\n\t" \
  "ds_read_b64_tr_b16 v[146:147], v161 offset:512\n\t" \
  "ds_read_b64_tr_b16 v[148:149], v166 offset:0\n\t" \
  "ds_read_b64_tr_b16 v[150:151], v166 offset:512\n\t" \
  "ds_read_b64_tr_b16 v[152:153], v11 offset:0\n\t" \
  "ds_read_b64_tr_b16 v[154:155], v11 offset:512\n\t" \
  "ds_read_b64_tr_b16 v[156:157], v15 offset:0\n\t" \
  "ds_read_b64_tr_b16 v[158:159], v15 offset:512\n\t" \
  "ds_read_b64_tr_b16 v[16:17], v161 offset:4096\n\t" \
  "ds_read_b64_tr_b16 v[18:19], v161 offset:4608\n\t" \
  "ds_read_b64_tr_b16 v[20:21], v166 offset:4096\n\t" \
  "ds_read_b64_tr_b16 v[22:23], v166 offset:4608\n\t" \
  "ds_read_b64_tr_b16 v[24:25], v11 offset:4096\n\t" \
  "ds_read_b64_tr_b16 v[26:27], v11 offset:4608\n\t" \
  "ds_read_b64_tr_b16 v[28:29], v15 offset:4096\n\t" \
  "ds_read_b64_tr_b16 v[30:31], v15 offset:4608\n\t" \
  "s_branch 10f\n\t" \
  "1:\n\t" \
  "s_sleep 2\n\t" \
  "0:\n\t" \
  "v_mov_b64 v[108:109], v[8:9]\n\t" \
  "s_mov_b64 exec, s[88:89]\n\t" \
  "v_mov_b64 v[108:109], 0\n\t" \
  "buffer_atomic_add_x2 v[108:109], v163, s[40:43], s93 offen sc0\n\t" \
  "s_mov_b64 exec, -1\n\t" \
  "10:\n\t" \
  "s_sub_u32 s50, s50, 1\n\t" \
  "s_waitcnt vmcnt(0)\n\t" \
  "v_cmp_ne_u64 vcc, v[8:9], v[108:109]\n\t" \
  "s_cbranch_scc1 4f\n\t" \
  "s_cbranch_vccnz 1b\n\t" \
  "4:\n\t" \
  "s_cmp_eq_u32 s92, 0\n\t" \
  "s_cbranch_scc1 2f\n\t" \
  "buffer_load_dwordx4 v[32:35], v160, s[40:43], s90 offen offset:0 sc1\n\t" \
  "s_add_u32 s90, s90, 0x20000\n\t" \
  "buffer_load_dwordx4 v[36:39], v160, s[40:43], s90 offen offset:0 sc1\n\t" \
  "s_add_u32 s90, s90, 0x20000\n\t" \
  "buffer_load_dwordx4 v[40:43], v160, s[40:43], s90 offen offset:0 sc1\n\t" \
  "s_add_u32 s90, s90, 0x20000\n\t" \
  "buffer_load_dwordx4 v[44:47], v160, s[40:43], s90 offen offset:0 sc1\n\t" \
  "s_add_u32 s90, s90, 0x20000\n\t" \
  "buffer_load_dwordx4 v[48:51], v160, s[40:43], s90 offen offset:0 sc1\n\t" \
  "s_add_u32 s90, s90, 0x20000\n\t" \
  "buffer_load_dwordx4 v[52:55], v160, s[40:43], s90 offen offset:0 sc1\n\t" \
  "s_add_u32 s90, s90, 0x20000\n\t" \
  "buffer_load_dwordx4 v[56:59], v160, s[40:43], s90 offen offset:0 sc1\n\t" \
  "s_add_u32 s90, s90, 0x20000\n\t" \
  "buffer_load_dwordx4 v[60:63], v160, s[40:43], s90 offen offset:0 sc1\n\t" \
  "s_add_u32 s90, s90, 0x20000\n\t" \
  "buffer_load_dwordx4 v[64:67], v160, s[40:43], s90 offen offset:0 sc1\n\t" \
  "s_add_u32 s90, s90, 0x20000\n\t" \
  "buffer_load_dwordx4 v[68:71], v160, s[40:43], s90 offen offset:0 sc1\n\t" \
  "s_add_u32 s90, s90, 0x20000\n\t" \
  "6:\n\t" \
  "s_cmp_eq_u32 s92, 1\n\t" \
  "s_waitcnt vmcnt(9) lgkmcnt(12)\n\t" \
  "v_mfma_f32_16x16x32_bf16 v[112:115], v[144:147], v[32:35], 0\n\t" \
  "ds_read_b64_tr_b16 v[0:1], v161 offset:8192\n\t" \
  "ds_read_b64_tr_b16 v[2:3], v161 offset:8704\n\t" \
  "v_mfma_f32_16x16x32_bf16 v[120:123], v[148:151], v[32:35], 0\n\t" \
  "ds_read_b64_tr_b16 v[4:5], v166 offset:8192\n\t" \
  "ds_read_b64_tr_b16 v[6:7], v166 offset:8704\n\t" \
  "s_waitcnt lgkmcnt(12)\n\t" \
  "v_mfma_f32_16x16x32_bf16 v[128:131], v[152:155], v[32:35], 0\n\t" \
  "ds_read_b64_tr_b16 v[144:145], v11 offset:8192\n\t" \
  "ds_read_b64_tr_b16 v[146:147], v11 offset:8704\n\t" \
  "v_mfma_f32_16x16x32_bf16 v[136:139], v[156:159], v[32:35], 0\n\t" \
  "ds_read_b64_tr_b16 v[148:149], v15 offset:8192\n\t" \
  "ds_read_b64_tr_b16 v[150:151], v15 offset:8704\n\t" \
  "s_waitcnt vmcnt(8) lgkmcnt(12)\n\t" \
  "v_mfma_f32_16x16x32_bf16 v[112:115], v[16:19], v[36:39], v[112:115]\n\t" \
  "ds_read_b64_tr_b16 v[152:153], v161 offset:12288\n\t" \
  "ds_read_b64_tr_b16 v[154:155], v161 offset:12800\n\t" \
  "v_mfma_f32_16x16x32_bf16 v[120:123], v[20:23], v[36:39], v[120:123]\n\t" \
  "ds_read_b64_tr_b16 v[156:157], v166 offset:12288\n\t" \
  "ds_read_b64_tr_b16 v[158:159], v166 offset:12800\n\t" \
  "s_waitcnt lgkmcnt(12)\n\t" \
  "v_mfma_f32_16x16x32_bf16 v[128:131], v[24:27], v[36:39], v[128:131]\n\t" \
  "ds_read_b64_tr_b16 v[16:17], v11 offset:12288\n\t" \
  "ds_read_b64_tr_b16 v[18:19], v11 offset:12800\n\t" \
  "v_mfma_f32_16x16x32_bf16 v[136:139], v[28:31], v[36:39], v[136:139]\n\t" \
  "ds_read_b64_tr_b16 v[20:21], v15 offset:12288\n\t" \
  "ds_read_b64_tr_b16 v[22:23], v15 offset:12800\n\t" \
  "s_waitcnt vmcnt(7) lgkmcnt(12)\n\t" \
  "v_mfma_f32_16x16x32_bf16 v[112:115], v[0:3], v[40:43], v[112:115]\n\t" \
  "ds_read_b64_tr_b16 v[24:25], v161 offset:16384\n\t" \
  "ds_read_b64_tr_b16 v[26:27], v161 offset:16896\n\t" \
  "v_mfma_f32_16x16x32_bf16 v[120:123], v[4:7], v[40:43], v[120:123]\n\t" \
  "ds_read_b64_tr_b16 v[28:29], v166 offset:16384\n\t" \
  "ds_read_b64_tr_b16 v[30:31], v166 offset:16896\n\t" \
  "s_waitcnt lgkmcnt(12)\n\t" \
  "v_mfma_f32_16x16x32_bf16 v[128:131], v[144:147], v[40:43], v[128:131]\n\t" \
  "ds_read_b64_tr_b16 v[0:1], v11 offset:16384\n\t" \
  "ds_read_b64_tr_b16 v[2:3], v11 offset:16896\n\t" \
  "v_mfma_f32_16x16x32_bf16 v[136:139], v[148:151], v[40:43], v[136:139]\n\t" \
  "ds_read_b64_tr_b16 v[4:5], v15 offset:16384\n\t" \
  "ds_read_b64_tr_b16 v[6:7], v15 offset:16896\n\t" \
  "s_waitcnt vmcnt(6) lgkmcnt(12)\n\t" \
  "v_mfma_f32_16x16x32_bf16 v[112:115], v[152:155], v[44:47], v[112:115]\n\t" \
  "ds_read_b64_tr_b16 v[144:145], v161 offset:20480\n\t" \
  "ds_read_b64_tr_b16 v[146:147], v161 offset:20992\n\t" \
  "v_mfma_f32_16x16x32_bf16 v[120:123], v[156:159], v[44:47], v[120:123]\n\t" \
  "ds_read_b64_tr_b16 v[148:149], v166 offset:20480\n\t" \
  "ds_read_b64_tr_b16 v[150:151], v166 offset:20992\n\t" \
  "s_waitcnt lgkmcnt(12)\n\t" \
  "v_mfma_f32_16x16x32_bf16 v[128:131], v[16:19], v[44:47], v[128:131]\n\t" \
  "ds_read_b64_tr_b16 v[152:153], v11 offset:20480\n\t" \
  "ds_read_b64_tr_b16 v[154:155], v11 offset:20992\n\t" \
  "v_mfma_f32_16x16x32_bf16 v[136:139], v[20:23], v[44:47], v[136:139]\n\t" \
  "ds_read_b64_tr_b16 v[156:157], v15 offset:20480\n\t" \
  "ds_read_b64_tr_b16 v[158:159], v15 offset:20992\n\t" \
  "s_waitcnt vmcnt(5) lgkmcnt(12)\n\t" \
  "v_mfma_f32_16x16x32_bf16 v[112:115], v[24:27], v[48:51], v[112:115]\n\t" \
  "ds_read_b64_tr_b16 v[16:17], v161 offset:24576\n\t" \
  "ds_read_b64_tr_b16 v[18:19], v161 offset:25088\n\t" \
  "v_mfma_f32_16x16x32_bf16 v[120:123], v[28:31], v[48:51], v[120:123]\n\t" \
  "ds_read_b64_tr_b16 v[20:21], v166 offset:24576\n\t" \
  "ds_read_b64_tr_b16 v[22:23], v166 offset:25088\n\t" \
  "s_waitcnt lgkmcnt(12)\n\t" \
  "v_mfma_f32_16x16x32_bf16 v[128:131], v[0:3], v[48:51], v[128:131]\n\t" \
  "ds_read_b64_tr_b16 v[24:25], v11 offset:24576\n\t" \
  "ds_read_b64_tr_b16 v[26:27], v11 offset:25088\n\t" \
  "v_mfma_f32_16x16x32_bf16 v[136:139], v[4:7], v[48:51], v[136:139]\n\t" \
  "ds_read_b64_tr_b16 v[28:29], v15 offset:24576\n\t" \
  "ds_read_b64_tr_b16 v[30:31], v15 offset:25088\n\t" \
  "s_waitcnt vmcnt(4) lgkmcnt(12)\n\t" \
  "v_mfma_f32_16x16x32_bf16 v[112:115], v[144:147], v[52:55], v[112:115]\n\t" \
  "ds_read_b64_tr_b16 v[0:1], v161 offset:28672\n\t" \
  "ds_read_b64_tr_b16 v[2:3], v161 offset:29184\n\t" \
  "v_mfma_f32_16x16x32_bf16 v[120:123], v[148:151], v[52:55], v[120:123]\n\t" \
  "ds_read_b64_tr_b16 v[4:5], v166 offset:28672\n\t" \
  "ds_read_b64_tr_b16 v[6:7], v166 offset:29184\n\t" \
  "s_waitcnt lgkmcnt(12)\n\t" \
  "v_mfma_f32_16x16x32_bf16 v[128:131], v[152:155], v[52:55], v[128:131]\n\t" \
  "ds_read_b64_tr_b16 v[144:145], v11 offset:28672\n\t" \
  "ds_read_b64_tr_b16 v[146:147], v11 offset:29184\n\t" \
  "v_mfma_f32_16x16x32_bf16 v[136:139], v[156:159], v[52:55], v[136:139]\n\t" \
  "ds_read_b64_tr_b16 v[148:149], v15 offset:28672\n\t" \
  "ds_read_b64_tr_b16 v[150:151], v15 offset:29184\n\t" \
  "s_waitcnt vmcnt(3) lgkmcnt(12)\n\t" \
  "v_mfma_f32_16x16x32_bf16 v[112:115], v[16:19], v[56:59], v[112:115]\n\t" \
  "ds_read_b64_tr_b16 v[152:153], v161 offset:32768\n\t" \
  "ds_read_b64_tr_b16 v[154:155], v161 offset:33280\n\t" \
  "v_mfma_f32_16x16x32_bf16 v[120:123], v[20:23], v[56:59], v[120:123]\n\t" \
  "ds_read_b64_tr_b16 v[156:157], v166 offset:32768\n\t" \
  "ds_read_b64_tr_b16 v[158:159], v166 offset:33280\n\t" \
  "s_cbranch_scc0 5f\n\t" \
  "s_barrier\n\t" \
  "5:\n\t" \
  "s_waitcnt lgkmcnt(12)\n\t" \
  "v_mfma_f32_16x16x32_bf16 v[128:131], v[24:27], v[56:59], v[128:131]\n\t" \
  "ds_read_b64_tr_b16 v[16:17], v11 offset:32768\n\t" \
  "ds_read_b64_tr_b16 v[18:19], v11 offset:33280\n\t" \
  "v_mfma_f32_16x16x32_bf16 v[136:139], v[28:31], v[56:59], v[136:139]\n\t" \
  "ds_read_b64_tr_b16 v[20:21], v15 offset:32768\n\t" \
  "ds_read_b64_tr_b16 v[22:23], v15 offset:33280\n\t" \
  "s_waitcnt vmcnt(2) lgkmcnt(12)\n\t" \
  "v_mfma_f32_16x16x32_bf16 v[112:115], v[0:3], v[60:63], v[112:115]\n\t" \
  "ds_read_b64_tr_b16 v[24:25], v161 offset:36864\n\t" \
  "ds_read_b64_tr_b16 v[26:27], v161 offset:37376\n\t" \
  "v_mfma_f32_16x16x32_bf16 v[120:123], v[4:7], v[60:63], v[120:123]\n\t" \
  "ds_read_b64_tr_b16 v[28:29], v166 offset:36864\n\t" \
  "ds_read_b64_tr_b16 v[30:31], v166 offset:37376\n\t" \
  "s_waitcnt lgkmcnt(12)\n\t" \
  "v_mfma_f32_16x16x32_bf16 v[128:131], v[144:147], v[60:63], v[128:131]\n\t" \
  "ds_read_b64_tr_b16 v[0:1], v11 offset:36864\n\t" \
  "ds_read_b64_tr_b16 v[2:3], v11 offset:37376\n\t" \
  "v_mfma_f32_16x16x32_bf16 v[136:139], v[148:151], v[60:63], v[136:139]\n\t" \
  "ds_read_b64_tr_b16 v[4:5], v15 offset:36864\n\t" \
  "ds_read_b64_tr_b16 v[6:7], v15 offset:37376\n\t" \
  "s_waitcnt vmcnt(1) lgkmcnt(12)\n\t" \
  "v_mfma_f32_16x16x32_bf16 v[112:115], v[152:155], v[64:67], v[112:115]\n\t" \
  "v_mfma_f32_16x16x32_bf16 v[120:123], v[156:159], v[64:67], v[120:123]\n\t" \
  "s_waitcnt lgkmcnt(8)\n\t" \
  "v_mfma_f32_16x16x32_bf16 v[128:131], v[16:19], v[64:67], v[128:131]\n\t" \
  "v_mfma_f32_16x16x32_bf16 v[136:139], v[20:23], v[64:67], v[136:139]\n\t" \
  "s_waitcnt vmcnt(0) lgkmcnt(4)\n\t" \
  "v_mfma_f32_16x16x32_bf16 v[112:115], v[24:27], v[68:71], v[112:115]\n\t" \
  "v_mfma_f32_16x16x32_bf16 v[120:123], v[28:31], v[68:71], v[120:123]\n\t" \
  "s_waitcnt lgkmcnt(0)\n\t" \
  "v_mfma_f32_16x16x32_bf16 v[128:131], v[0:3], v[68:71], v[128:131]\n\t" \
  "v_mfma_f32_16x16x32_bf16 v[136:139], v[4:7], v[68:71], v[136:139]\n\t" \
  "s_mov_b64 vcc, -1\n\t" \
  "s_branch 8f\n\t" \
  "2:\n\t" \
  "buffer_load_dwordx4 v[32:35], v160, s[40:43], s90 offen offset:0 sc1\n\t" \
  "buffer_load_dwordx4 v[36:39], v160, s[40:43], s90 offen offset:256 sc1\n\t" \
  "s_add_u32 s90, s90, 0x20000\n\t" \
  "buffer_load_dwordx4 v[40:43], v160, s[40:43], s90 offen offset:0 sc1\n\t" \
  "buffer_load_dwordx4 v[44:47], v160, s[40:43], s90 offen offset:256 sc1\n\t" \
  "s_add_u32 s90, s90, 0x20000\n\t" \
  "buffer_load_dwordx4 v[48:51], v160, s[40:43], s90 offen offset:0 sc1\n\t" \
  "buffer_load_dwordx4 v[52:55], v160, s[40:43], s90 offen offset:256 sc1\n\t" \
  "s_add_u32 s90, s90, 0x20000\n\t" \
  "buffer_load_dwordx4 v[56:59], v160, s[40:43], s90 offen offset:0 sc1\n\t" \
  "buffer_load_dwordx4 v[60:63], v160, s[40:43], s90 offen offset:256 sc1\n\t" \
  "s_add_u32 s90, s90, 0x20000\n\t" \
  "buffer_load_dwordx4 v[64:67], v160, s[40:43], s90 offen offset:0 sc1\n\t" \
  "buffer_load_dwordx4 v[68:71], v160, s[40:43], s90 offen offset:256 sc1\n\t" \
  "s_add_u32 s90, s90, 0x20000\n\t" \
  "buffer_load_dwordx4 v[72:75], v160, s[40:43], s90 offen offset:0 sc1\n\t" \
  "buffer_load_dwordx4 v[76:79], v160, s[40:43], s90 offen offset:256 sc1\n\t" \
  "s_add_u32 s90, s90, 0x20000\n\t" \
  "buffer_load_dwordx4 v[80:83], v160, s[40:43], s90 offen offset:0 sc1\n\t" \
  "buffer_load_dwordx4 v[84:87], v160, s[40:43], s90 offen offset:256 sc1\n\t" \
  "s_add_u32 s90, s90, 0x20000\n\t" \
  "s_waitcnt vmcnt(12) lgkmcnt(12)\n\t" \
  "v_mfma_f32_16x16x32_bf16 v[112:115], v[144:147], v[32:35], 0\n\t" \
  "ds_read_b64_tr_b16 v[0:1], v161 offset:8192\n\t" \
  "ds_read_b64_tr_b16 v[2:3], v161 offset:8704\n\t" \
  "v_mfma_f32_16x16x32_bf16 v[116:119], v[144:147], v[36:39], 0\n\t" \
  "ds_read_b64_tr_b16 v[4:5], v166 offset:8192\n\t" \
  "v_mfma_f32_16x16x32_bf16 v[124:127], v[148:151], v[36:39], 0\n\t" \
  "ds_read_b64_tr_b16 v[6:7], v166 offset:8704\n\t" \
  "v_mfma_f32_16x16x32_bf16 v[120:123], v[148:151], v[32:35], 0\n\t" \
  "buffer_load_dwordx4 v[88:91], v160, s[40:43], s90 offen offset:0 sc1\n\t" \
  "s_waitcnt lgkmcnt(12)\n\t" \
  "v_mfma_f32_16x16x32_bf16 v[128:131], v[152:155], v[32:35], 0\n\t" \
  "ds_read_b64_tr_b16 v[144:145], v11 offset:8192\n\t" \
  "ds_read_b64_tr_b16 v[146:147], v11 offset:8704\n\t" \
  "v_mfma_f32_16x16x32_bf16 v[132:135], v[152:155], v[36:39], 0\n\t" \
  "ds_read_b64_tr_b16 v[148:149], v15 offset:8192\n\t" \
  "v_mfma_f32_16x16x32_bf16 v[140:143], v[156:159], v[36:39], 0\n\t" \
  "ds_read_b64_tr_b16 v[150:151], v15 offset:8704\n\t" \
  "v_mfma_f32_16x16x32_bf16 v[136:139], v[156:159], v[32:35], 0\n\t" \
  "buffer_load_dwordx4 v[92:95], v160, s[40:43], s90 offen offset:256 sc1\n\t" \
  "s_add_u32 s90, s90, 0x20000\n\t" \
  "s_waitcnt vmcnt(12) lgkmcnt(12)\n\t" \
  "v_mfma_f32_16x16x32_bf16 v[112:115], v[16:19], v[40:43], v[112:115]\n\t" \
  "ds_read_b64_tr_b16 v[152:153], v161 offset:12288\n\t" \
  "ds_read_b64_tr_b16 v[154:155], v161 offset:12800\n\t" \
  "v_mfma_f32_16x16x32_bf16 v[116:119], v[16:19], v[44:47], v[116:119]\n\t" \
  "ds_read_b64_tr_b16 v[156:157], v166 offset:12288\n\t" \
  "v_mfma_f32_16x16x32_bf16 v[124:127], v[20:23], v[44:47], v[124:127]\n\t" \
  "ds_read_b64_tr_b16 v[158:159], v166 offset:12800\n\t" \
  "v_mfma_f32_16x16x32_bf16 v[120:123], v[20:23], v[40:43], v[120:123]\n\t" \
  "buffer_load_dwordx4 v[96:99], v160, s[40:43], s90 offen offset:0 sc1\n\t" \
  "s_waitcnt lgkmcnt(12)\n\t" \
  "v_mfma_f32_16x16x32_bf16 v[128:131], v[24:27], v[40:43], v[128:131]\n\t" \
  "ds_read_b64_tr_b16 v[16:17], v11 offset:12288\n\t" \
  "ds_read_b64_tr_b16 v[18:19], v11 offset:12800\n\t" \
  "v_mfma_f32_16x16x32_bf16 v[132:135], v[24:27], v[44:47], v[132:135]\n\t" \
  "ds_read_b64_tr_b16 v[20:21], v15 offset:12288\n\t" \
  "v_mfma_f32_16x16x32_bf16 v[140:143], v[28:31], v[44:47], v[140:143]\n\t" \
  "ds_read_b64_tr_b16 v[22:23], v15 offset:12800\n\t" \
  "v_mfma_f32_16x16x32_bf16 v[136:139], v[28:31], v[40:43], v[136:139]\n\t" \
  "buffer_load_dwordx4 v[100:103], v160, s[40:43], s90 offen offset:256 sc1\n\t" \
  "s_add_u32 s90, s90, 0x20000\n\t" \
  "s_waitcnt vmcnt(12) lgkmcnt(12)\n\t" \
  "v_mfma_f32_16x16x32_bf16 v[112:115], v[0:3], v[48:51], v[112:115]\n\t" \
  "ds_read_b64_tr_b16 v[24:25], v161 offset:16384\n\t" \
  "ds_read_b64_tr_b16 v[26:27], v161 offset:16896\n\t" \
  "v_mfma_f32_16x16x32_bf16 v[116:119], v[0:3], v[52:55], v[116:119]\n\t" \
  "ds_read_b64_tr_b16 v[28:29], v166 offset:16384\n\t" \
  "v_mfma_f32_16x16x32_bf16 v[124:127], v[4:7], v[52:55], v[124:127]\n\t" \
  "ds_read_b64_tr_b16 v[30:31], v166 offset:16896\n\t" \
  "v_mfma_f32_16x16x32_bf16 v[120:123], v[4:7], v[48:51], v[120:123]\n\t" \
  "buffer_load_dwordx4 v[104:107], v160, s[40:43], s90 offen offset:0 sc1\n\t" \
  "s_waitcnt lgkmcnt(12)\n\t" \
  "v_mfma_f32_16x16x32_bf16 v[128:131], v[144:147], v[48:51], v[128:131]\n\t" \
  "ds_read_b64_tr_b16 v[0:1], v11 offset:16384\n\t" \
  "ds_read_b64_tr_b16 v[2:3], v11 offset:16896\n\t" \
  "v_mfma_f32_16x16x32_bf16 v[132:135], v[144:147], v[52:55], v[132:135]\n\t" \
  "ds_read_b64_tr_b16 v[4:5], v15 offset:16384\n\t" \
  "v_mfma_f32_16x16x32_bf16 v[140:143], v[148:151], v[52:55], v[140:143]\n\t" \
  "ds_read_b64_tr_b16 v[6:7], v15 offset:16896\n\t" \
  "v_mfma_f32_16x16x32_bf16 v[136:139], v[148:151], v[48:51], v[136:139]\n\t" \
  "buffer_load_dwordx4 v[108:111], v160, s[40:43], s90 offen offset:256 sc1\n\t" \
  "s_add_u32 s90, s90, 0x20000\n\t" \
  "s_waitcnt vmcnt(12) lgkmcnt(12)\n\t" \
  "v_mfma_f32_16x16x32_bf16 v[112:115], v[152:155], v[56:59], v[112:115]\n\t" \
  "ds_read_b64_tr_b16 v[144:145], v161 offset:20480\n\t" \
  "ds_read_b64_tr_b16 v[146:147], v161 offset:20992\n\t" \
  "v_mfma_f32_16x16x32_bf16 v[116:119], v[152:155], v[60:63], v[116:119]\n\t" \
  "ds_read_b64_tr_b16 v[148:149], v166 offset:20480\n\t" \
  "v_mfma_f32_16x16x32_bf16 v[124:127], v[156:159], v[60:63], v[124:127]\n\t" \
  "ds_read_b64_tr_b16 v[150:151], v166 offset:20992\n\t" \
  "v_mfma_f32_16x16x32_bf16 v[120:123], v[156:159], v[56:59], v[120:123]\n\t" \
  "s_waitcnt lgkmcnt(12)\n\t" \
  "v_mfma_f32_16x16x32_bf16 v[128:131], v[16:19], v[56:59], v[128:131]\n\t" \
  "ds_read_b64_tr_b16 v[152:153], v11 offset:20480\n\t" \
  "ds_read_b64_tr_b16 v[154:155], v11 offset:20992\n\t" \
  "v_mfma_f32_16x16x32_bf16 v[132:135], v[16:19], v[60:63], v[132:135]\n\t" \
  "ds_read_b64_tr_b16 v[156:157], v15 offset:20480\n\t" \
  "v_mfma_f32_16x16x32_bf16 v[140:143], v[20:23], v[60:63], v[140:143]\n\t" \
  "ds_read_b64_tr_b16 v[158:159], v15 offset:20992\n\t" \
  "v_mfma_f32_16x16x32_bf16 v[136:139], v[20:23], v[56:59], v[136:139]\n\t" \
  "s_waitcnt vmcnt(10) lgkmcnt(12)\n\t" \
  "v_mfma_f32_16x16x32_bf16 v[112:115], v[24:27], v[64:67], v[112:115]\n\t" \
  "ds_read_b64_tr_b16 v[16:17], v161 offset:24576\n\t" \
  "ds_read_b64_tr_b16 v[18:19], v161 offset:25088\n\t" \
  "v_mfma_f32_16x16x32_bf16 v[116:119], v[24:27], v[68:71], v[116:119]\n\t" \
  "ds_read_b64_tr_b16 v[20:21], v166 offset:24576\n\t" \
  "v_mfma_f32_16x16x32_bf16 v[124:127], v[28:31], v[68:71], v[124:127]\n\t" \
  "ds_read_b64_tr_b16 v[22:23], v166 offset:25088\n\t" \
  "v_mfma_f32_16x16x32_bf16 v[120:123], v[28:31], v[64:67], v[120:123]\n\t" \
  "s_waitcnt lgkmcnt(12)\n\t" \
  "v_mfma_f32_16x16x32_bf16 v[128:131], v[0:3], v[64:67], v[128:131]\n\t" \
  "ds_read_b64_tr_b16 v[24:25], v11 offset:24576\n\t" \
  "ds_read_b64_tr_b16 v[26:27], v11 offset:25088\n\t" \
  "v_mfma_f32_16x16x32_bf16 v[132:135], v[0:3], v[68:71], v[132:135]\n\t" \
  "ds_read_b64_tr_b16 v[28:29], v15 offset:24576\n\t" \
  "v_mfma_f32_16x16x32_bf16 v[140:143], v[4:7], v[68:71], v[140:143]\n\t" \
  "ds_read_b64_tr_b16 v[30:31], v15 offset:25088\n\t" \
  "v_mfma_f32_16x16x32_bf16 v[136:139], v[4:7], v[64:67], v[136:139]\n\t" \
  "3:\n\t" \
  "s_cmp_eq_u32 s92, 0\n\t" \
  "s_mov_b64 vcc, 0\n\t" \
  "s_waitcnt vmcnt(8) lgkmcnt(12)\n\t" \
  "v_mfma_f32_16x16x32_bf16 v[112:115], v[144:147], v[72:75], v[112:115]\n\t" \
  "ds_read_b64_tr_b16 v[0:1], v161 offset:28672\n\t" \
  "ds_read_b64_tr_b16 v[2:3], v161 offset:29184\n\t" \
  "v_mfma_f32_16x16x32_bf16 v[116:119], v[144:147], v[76:79], v[116:119]\n\t" \
  "ds_read_b64_tr_b16 v[4:5], v166 offset:28672\n\t" \
  "v_mfma_f32_16x16x32_bf16 v[124:127], v[148:151], v[76:79], v[124:127]\n\t" \
  "ds_read_b64_tr_b16 v[6:7], v166 offset:29184\n\t" \
  "v_mfma_f32_16x16x32_bf16 v[120:123], v[148:151], v[72:75], v[120:123]\n\t" \
  "s_cbranch_scc0 5f\n\t" \
  "s_barrier\n\t" \
  "5:\n\t" \
  "s_waitcnt lgkmcnt(12)\n\t" \
  "v_mfma_f32_16x16x32_bf16 v[128:131], v[152:155], v[72:75], v[128:131]\n\t" \
  "ds_read_b64_tr_b16 v[144:145], v11 offset:28672\n\t" \
  "ds_read_b64_tr_b16 v[146:147], v11 offset:29184\n\t" \
  "v_mfma_f32_16x16x32_bf16 v[132:135], v[152:155], v[76:79], v[132:135]\n\t" \
  "ds_read_b64_tr_b16 v[148:149], v15 offset:28672\n\t" \
  "v_mfma_f32_16x16x32_bf16 v[140:143], v[156:159], v[76:79], v[140:143]\n\t" \
  "ds_read_b64_tr_b16 v[150:151], v15 offset:29184\n\t" \
  "v_mfma_f32_16x16x32_bf16 v[136:139], v[156:159], v[72:75], v[136:139]\n\t" \
  "s_waitcnt vmcnt(6) lgkmcnt(12)\n\t" \
  "v_mfma_f32_16x16x32_bf16 v[112:115], v[16:19], v[80:83], v[112:115]\n\t" \
  "ds_read_b64_tr_b16 v[152:153], v161 offset:32768\n\t" \
  "ds_read_b64_tr_b16 v[154:155], v161 offset:33280\n\t" \
  "v_mfma_f32_16x16x32_bf16 v[116:119], v[16:19], v[84:87], v[116:119]\n\t" \
  "ds_read_b64_tr_b16 v[156:157], v166 offset:32768\n\t" \
  "v_mfma_f32_16x16x32_bf16 v[124:127], v[20:23], v[84:87], v[124:127]\n\t" \
  "ds_read_b64_tr_b16 v[158:159], v166 offset:33280\n\t" \
  "v_mfma_f32_16x16x32_bf16 v[120:123], v[20:23], v[80:83], v[120:123]\n\t" \
  "s_waitcnt lgkmcnt(12)\n\t" \
  "v_mfma_f32_16x16x32_bf16 v[128:131], v[24:27], v[80:83], v[128:131]\n\t" \
  "ds_read_b64_tr_b16 v[16:17], v11 offset:32768\n\t" \
  "ds_read_b64_tr_b16 v[18:19], v11 offset:33280\n\t" \
  "v_mfma_f32_16x16x32_bf16 v[132:135], v[24:27], v[84:87], v[132:135]\n\t" \
  "ds_read_b64_tr_b16 v[20:21], v15 offset:32768\n\t" \
  "v_mfma_f32_16x16x32_bf16 v[140:143], v[28:31], v[84:87], v[140:143]\n\t" \
  "ds_read_b64_tr_b16 v[22:23], v15 offset:33280\n\t" \
  "v_mfma_f32_16x16x32_bf16 v[136:139], v[28:31], v[80:83], v[136:139]\n\t" \
  "s_waitcnt vmcnt(4) lgkmcnt(12)\n\t" \
  "v_mfma_f32_16x16x32_bf16 v[112:115], v[0:3], v[88:91], v[112:115]\n\t" \
  "ds_read_b64_tr_b16 v[24:25], v161 offset:36864\n\t" \
  "ds_read_b64_tr_b16 v[26:27], v161 offset:37376\n\t" \
  "v_mfma_f32_16x16x32_bf16 v[116:119], v[0:3], v[92:95], v[116:119]\n\t" \
  "ds_read_b64_tr_b16 v[28:29], v166 offset:36864\n\t" \
  "v_mfma_f32_16x16x32_bf16 v[124:127], v[4:7], v[92:95], v[124:127]\n\t" \
  "ds_read_b64_tr_b16 v[30:31], v166 offset:37376\n\t" \
  "v_mfma_f32_16x16x32_bf16 v[120:123], v[4:7], v[88:91], v[120:123]\n\t" \
  "s_waitcnt lgkmcnt(12)\n\t" \
  "v_mfma_f32_16x16x32_bf16 v[128:131], v[144:147], v[88:91], v[128:131]\n\t" \
  "ds_read_b64_tr_b16 v[0:1], v11 offset:36864\n\t" \
  "ds_read_b64_tr_b16 v[2:3], v11 offset:37376\n\t" \
  "v_mfma_f32_16x16x32_bf16 v[132:135], v[144:147], v[92:95], v[132:135]\n\t" \
  "ds_read_b64_tr_b16 v[4:5], v15 offset:36864\n\t" \
  "v_mfma_f32_16x16x32_bf16 v[140:143], v[148:151], v[92:95], v[140:143]\n\t" \
  "ds_read_b64_tr_b16 v[6:7], v15 offset:37376\n\t" \
  "v_mfma_f32_16x16x32_bf16 v[136:139], v[148:151], v[88:91], v[136:139]\n\t" \
  "s_waitcnt vmcnt(2) lgkmcnt(12)\n\t" \
  "v_mfma_f32_16x16x32_bf16 v[112:115], v[152:155], v[96:99], v[112:115]\n\t" \
  "v_mfma_f32_16x16x32_bf16 v[116:119], v[152:155], v[100:103], v[116:119]\n\t" \
  "v_mfma_f32_16x16x32_bf16 v[124:127], v[156:159], v[100:103], v[124:127]\n\t" \
  "v_mfma_f32_16x16x32_bf16 v[120:123], v[156:159], v[96:99], v[120:123]\n\t" \
  "s_waitcnt lgkmcnt(8)\n\t" \
  "v_mfma_f32_16x16x32_bf16 v[128:131], v[16:19], v[96:99], v[128:131]\n\t" \
  "v_mfma_f32_16x16x32_bf16 v[132:135], v[16:19], v[100:103], v[132:135]\n\t" \
  "v_mfma_f32_16x16x32_bf16 v[140:143], v[20:23], v[100:103], v[140:143]\n\t" \
  "v_mfma_f32_16x16x32_bf16 v[136:139], v[20:23], v[96:99], v[136:139]\n\t" \
  "s_waitcnt vmcnt(0) lgkmcnt(4)\n\t" \
  "v_mfma_f32_16x16x32_bf16 v[112:115], v[24:27], v[104:107], v[112:115]\n\t" \
  "v_mfma_f32_16x16x32_bf16 v[116:119], v[24:27], v[108:111], v[116:119]\n\t" \
  "v_mfma_f32_16x16x32_bf16 v[124:127], v[28:31], v[108:111], v[124:127]\n\t" \
  "v_mfma_f32_16x16x32_bf16 v[120:123], v[28:31], v[104:107], v[120:123]\n\t" \
  "s_waitcnt lgkmcnt(0)\n\t" \
  "v_mfma_f32_16x16x32_bf16 v[128:131], v[0:3], v[104:107], v[128:131]\n\t" \
  "v_mfma_f32_16x16x32_bf16 v[132:135], v[0:3], v[108:111], v[132:135]\n\t" \
  "v_mfma_f32_16x16x32_bf16 v[140:143], v[4:7], v[108:111], v[140:143]\n\t" \
  "v_mfma_f32_16x16x32_bf16 v[136:139], v[4:7], v[104:107], v[136:139]\n\t" \
  "8:\n\t" \
  "s_cmp_lt_u32 s92, 2\n\t" \
  "s_setprio 0\n\t" \
  "s_barrier\n\t" \
  "v_add_u32 v12, 81920, v164\n\t" \
  "v_add_u32 v13, 81920, v14\n\t" \
  "s_cbranch_scc1 9f\n\t" \
  "s_nop 3\n\t" \
  "ds_write_b128 v162, v[112:115] offset:0\n\t" \
  "ds_write_b128 v10, v[120:123] offset:0\n\t" \
  "ds_write_b128 v162, v[128:131] offset:128\n\t" \
  "ds_write_b128 v10, v[136:139] offset:128\n\t" \
  "s_cbranch_vccnz 5f\n\t" \
  "ds_write_b128 v162, v[116:119] offset:4096\n\t" \
  "ds_write_b128 v10, v[124:127] offset:4096\n\t" \
  "ds_write_b128 v162, v[132:135] offset:4224\n\t" \
  "ds_write_b128 v10, v[140:143] offset:4224\n\t" \
  "5:\n\t" \
  "s_waitcnt lgkmcnt(0)\n\t" \
  "s_barrier\n\t" \
  "ds_read_b128 v[32:35], v164\n\t" \
  "ds_read_b128 v[36:39], v14\n\t" \
  "ds_read_b128 v[40:43], v164 offset:24576\n\t" \
  "ds_read_b128 v[44:47], v14 offset:24576\n\t" \
  "s_waitcnt lgkmcnt(0)\n\t" \
  "s_branch 8f\n\t" \
  "9:\n\t" \
  "ds_read_b128 v[32:35], v164\n\t" \
  "ds_read_b128 v[36:39], v14\n\t" \
  "ds_read_b128 v[40:43], v164 offset:24576\n\t" \
  "ds_read_b128 v[44:47], v14 offset:24576\n\t" \
  "ds_write_b128 v162, v[112:115] offset:0\n\t" \
  "ds_write_b128 v10, v[120:123] offset:0\n\t" \
  "ds_write_b128 v162, v[128:131] offset:128\n\t" \
  "ds_write_b128 v10, v[136:139] offset:128\n\t" \
  "s_cbranch_vccnz 5f\n\t" \
  "ds_write_b128 v162, v[116:119] offset:4096\n\t" \
  "ds_write_b128 v10, v[124:127] offset:4096\n\t" \
  "ds_write_b128 v162, v[132:135] offset:4224\n\t" \
  "ds_write_b128 v10, v[140:143] offset:4224\n\t" \
  "5:\n\t" \
  "s_waitcnt lgkmcnt(4)\n\t" \
  "8:\n\t" \
  "v_pk_add_f32 v[32:33], v[32:33], v[40:41]\n\t" \
  "v_pk_add_f32 v[34:35], v[34:35], v[42:43]\n\t" \
  "v_pk_add_f32 v[36:37], v[36:37], v[44:45]\n\t" \
  "v_pk_add_f32 v[38:39], v[38:39], v[46:47]\n\t" \
  "v_cvt_pk_bf16_f32 v32, v32, v33\n\t" \
  "v_cvt_pk_bf16_f32 v33, v34, v35\n\t" \
  "v_cvt_pk_bf16_f32 v34, v36, v37\n\t" \
  "v_cvt_pk_bf16_f32 v35, v38, v39\n\t" \
  "buffer_store_dwordx4 v[32:35], v165, s[84:87], s94 offen sc1\n\t" \
  "s_waitcnt lgkmcnt(0)\n\t" \
  "s_barrier\n\t" \
  "ds_read_b128 v[48:51], v12\n\t" \
  "ds_read_b128 v[52:55], v13\n\t" \
  "ds_read_b128 v[56:59], v12 offset:24576\n\t" \
  "ds_read_b128 v[60:63], v13 offset:24576\n\t" \
  "s_waitcnt lgkmcnt(0)\n\t" \
  "v_pk_add_f32 v[48:49], v[48:49], v[56:57]\n\t" \
  "v_pk_add_f32 v[50:51], v[50:51], v[58:59]\n\t" \
  "v_pk_add_f32 v[52:53], v[52:53], v[60:61]\n\t" \
  "v_pk_add_f32 v[54:55], v[54:55], v[62:63]\n\t" \
  "v_cvt_pk_bf16_f32 v48, v48, v49\n\t" \
  "v_cvt_pk_bf16_f32 v49, v50, v51\n\t" \
  "v_cvt_pk_bf16_f32 v50, v52, v53\n\t" \
  "v_cvt_pk_bf16_f32 v51, v54, v55\n\t" \
  "buffer_store_dwordx4 v[48:51], v165, s[84:87], s95 offen sc1\n\t" \
  ""

constexpr int SEQ = 2048, DM = 640;
constexpr float QSCALE = 0.22360679774997896f * 1.4426950408889634f;
constexpr unsigned IMG = 80u * 2048u * 16u;
constexpr unsigned WS_Q = 0, WS_K = IMG, WS_V = 2 * IMG, WS_O = 3 * IMG, WS_BR = 4 * IMG;
constexpr unsigned WS_EPOCH = 2 * WS_BR, WS_GF = WS_EPOCH + 4096, WS_OF = WS_GF + 2 * 32 * 32 * 8, WS_BYTES = WS_OF + 256 * 128;
constexpr unsigned OOB = 0x40000000u;
constexpr int SC1 = 16;
constexpr int SLOT = 22528, L_QB = 3 * SLOT, L_B = 81920, L_MRG = L_B + 45056, L_QA = L_MRG + 24576, LDS_BYTES = 163840;
#define FLAG_AT 12
#define CHECK_AT 18
#define SAMPLE_AT 17
#define IDLE_SLEEP 70
#define PFS 128
#define W_SLEEP 14
#define PRE_SLEEP 8

__host__ __device__ constexpr int perm32(int r) { return (r & 19) | ((r & 4) << 1) | ((r & 8) >> 1); }
__device__ __forceinline__ unsigned gflag(int br, int rowu, int colu) { return WS_GF + ((br * 32 + colu) * 32 + rowu) * 8; }
__device__ __forceinline__ unsigned octr(int br, int qt, int head) { return WS_OF + ((br * 64 + qt) * 2 + (head >> 4)) * 128; }
__device__ __forceinline__ unsigned pk2(float a, float b) { f32x2 v = {a, b}; return __builtin_bit_cast(unsigned, __builtin_convertvector(v, bf16x2)); }
__device__ __forceinline__ u32x4 make_srd(const void* p, unsigned bytes) {
  uint64_t a = (uint64_t)p; u32x4 s; s[0] = (unsigned)a; s[1] = (unsigned)(a >> 32) & 0xffffu; s[2] = bytes; s[3] = 0x00020000u; return s; }
__device__ __forceinline__ rsrc_t make_rsrc(const void* p, unsigned bytes) { return __builtin_amdgcn_make_buffer_rsrc((void*)p, 0, bytes, 0x00020000); }
__device__ __forceinline__ void dma16(u32x4 srd, unsigned voff, unsigned soff, unsigned ldsdst) {
  asm volatile("s_mov_b32 m0, %3\n\ts_nop 0\n\tbuffer_load_dwordx4 %0, %1, %2 offen lds" :: "v"(voff), "s"(srd), "s"(soff), "s"(ldsdst) : "memory"); }
__device__ __forceinline__ void dma16c(u32x4 srd, unsigned voff, unsigned soff, unsigned ldsdst) {
  asm volatile("s_mov_b32 m0, %3\n\ts_nop 0\n\tbuffer_load_dwordx4 %0, %1, %2 offen sc1 lds" :: "v"(voff), "s"(srd), "s"(soff), "s"(ldsdst) : "memory"); }
__device__ __forceinline__ int fresh_lane() { int l; asm volatile("v_mbcnt_lo_u32_b32 %0, -1, 0\n\tv_mbcnt_hi_u32_b32 %0, -1, %0" : "=v"(l)); return l; }
__device__ __forceinline__ u64 opaque0() { u64 z = 0; asm volatile("" : "+v"(z)); return z; }
__device__ __forceinline__ int later(int x) { asm volatile("" : "+s"(x)); return x; }
#define MFMA16(a, b, c) __builtin_amdgcn_mfma_f32_32x32x16_bf16(a, b, c, 0, 0, 0)
#define MFMA8(a, b, c) __builtin_amdgcn_mfma_f32_32x32x8bf16_1k(a, b, c, 0, 0, 0)
#define BAR() asm volatile("s_barrier" ::: "memory")
#define WAITV(n) asm volatile("s_waitcnt vmcnt(" #n ")" ::: "memory")
#define V10(a) "v" #a "0", "v" #a "1", "v" #a "2", "v" #a "3", "v" #a "4", "v" #a "5", "v" #a "6", "v" #a "7", "v" #a "8", "v" #a "9"

__device__ __forceinline__ u64 ld_flag(char* ws, unsigned off) { return __hip_atomic_load((GLB_AS u64*)(ws + off), __ATOMIC_RELAXED, __HIP_MEMORY_SCOPE_AGENT); }
__device__ __forceinline__ void st_flag(char* ws, unsigned off, u64 tag, int lane) {
  if (lane == 0) __hip_atomic_store((GLB_AS u64*)(ws + off), tag, __ATOMIC_RELAXED, __HIP_MEMORY_SCOPE_AGENT); }
__device__ __forceinline__ void arrive(char* ws, unsigned off, int lane) {
  if (lane == 0) (void)__hip_atomic_fetch_add((GLB_AS u64*)(ws + off), 1ull, __ATOMIC_RELAXED, __HIP_MEMORY_SCOPE_AGENT); }
__device__ __forceinline__ void poll3(char* ws, unsigned f0, unsigned f1, unsigned f2, u64 tag) {
#pragma unroll 1
  for (int spin = 0; spin < (1 << 20); ++spin) {
    const u64 a = ld_flag(ws, f0), b = ld_flag(ws, f1), c = ld_flag(ws, f2);
    if (__all(a == tag && b == tag && c == tag)) break;
    __builtin_amdgcn_s_sleep(8);
  }
  asm volatile("" ::: "memory");
}
__device__ __forceinline__ void poll1(char* ws, unsigned f0, u64 tag) {
#pragma unroll 1
  for (int spin = 0; spin < (1 << 20); ++spin) {
    if (__all(ld_flag(ws, f0) == tag)) break;
    __builtin_amdgcn_s_sleep(8);
  }
  asm volatile("" ::: "memory");
}

struct Tables { float mask[4][64][4]; unsigned tr[16][64][4]; unsigned tw[12][64][2]; };
constexpr Tables make_tables() {
  Tables t{};
  for (int g = 0; g < 4; ++g) for (int l = 0; l < 64; ++l) for (int e = 0; e < 4; ++e) {
    const bool le = (e + 8 * g + 4 * (l >> 5)) <= (l & 31);
    t.mask[g][l][e] = le ? 0.f : -__builtin_huge_valf(); }
  for (int v = 0; v < 16; ++v) for (int l = 0; l < 64; ++l) {
    const int br = (v >> 2) & 1, kh = ((v >> 1) ^ (v >> 3)) & 1, rt = v >= 8 ? 4 + (v & 1) : 2 * (v & 1), g = l >> 4, q = (l >> 2) & 3;
    t.tr[v][l][0] = br * WS_BR + WS_O + (kh * 40 + g) * 32768 + (16 * rt + (l & 15)) * 16;
    t.tr[v][l][1] = br * L_B + kh * 40960 + (8 * g + q) * 128 + (((q & 2) | (g & 1)) << 5) + (l & 3) * 8;
    t.tr[v][l][2] = br * L_B + kh * 24576 + (16 * rt + (l & 15)) * 256 + ((g ^ (l & 7)) << 4);
    t.tr[v][l][3] = WS_OF + ((br * 64 + (l & 3)) * 2 + kh) * 128;
  }
  for (int w = 0; w < 12; ++w) for (int l = 0; l < 64; ++l) {
    const int tid = 64 * w + l;
    t.tw[w][l][0] = (tid >> 3) * 256 + (((2 * (tid & 7)) ^ ((tid >> 3) & 7)) << 4);
    t.tw[w][l][1] = (tid >> 3) * 2560 + (tid & 7) * 16;
  }
  return t; }
static __device__ const Tables TAB = make_tables();

struct QFrag { bf16x8 m; s16x4 t; };
__device__ __forceinline__ int ccol3(int head, int i) { return 5 * (head >> 1) + (i == 2 ? 2 : 3 * (head & 1) + i); }
__device__ __forceinline__ QFrag load_frag(rsrc_t rw, unsigned img, int head, int tok0, int lane) {
  QFrag f; const int hh = lane >> 5, tok = tok0 + (lane & 31);
  f.m = __builtin_bit_cast(bf16x8, __builtin_amdgcn_raw_buffer_load_b128(rw, (ccol3(head, 0) + hh) * 32768 + tok * 16, img, SC1));
  s16x4 t = __builtin_bit_cast(s16x4, __builtin_amdgcn_raw_buffer_load_b64(rw, ccol3(head, 2) * 32768 + tok * 16 + (head & 1) * 8, img, SC1));
  f.t = hh ? (s16x4){0, 0, 0, 0} : t; return f; }
__device__ __forceinline__ int vpos(int head, int r) { int d = r < 16 ? perm32(r) : r; return (head & 1) ? (d < 16 ? d + 4 : d - 16) : d; }
__device__ __forceinline__ f32x2 halves(float v) { auto t = __builtin_amdgcn_permlane32_swap(__float_as_uint(v), __float_as_uint(v), false, false); return (f32x2){__uint_as_float(t[0]), __uint_as_float(t[1])}; }
__device__ __forceinline__ f32x16 pv(u32x4 v, f32x8 p, f32x16 o) {
  return __builtin_amdgcn_smfmac_f32_32x32x32_bf16(__builtin_bit_cast(bf16x8, v), __builtin_bit_cast(bf16x16, p), o, 0xddddu, 0, 0); }
__device__ __forceinline__ f32x16 attn_exact(rsrc_t rw, int br, int head, int qt, int lane) {
  const int hh = lane >> 5, r = lane & 31; const unsigned bb = br * WS_BR;
  const int t_hi = br ? qt : 63, t_lo = br ? (qt >= 8 ? qt - 8 : 0) : 0;
  const QFrag q = load_frag(rw, bb + WS_Q, head, 32 * qt, lane);
  f32x16 o = {}; float m = -INFINITY;
  for (int t = t_hi; t >= t_lo; --t) {
    QFrag k = load_frag(rw, bb + WS_K, head, 32 * t, lane);
    const unsigned vo = (4 * t + hh) * 10240 + head * 320 + vpos(head, r < 20 ? r : 0) * 16;
    u32x4 v0 = __builtin_amdgcn_raw_buffer_load_b128(rw, vo, bb + WS_V, SC1), v1 = __builtin_amdgcn_raw_buffer_load_b128(rw, vo + 20480, bb + WS_V, SC1);
    if (r >= 20) { const unsigned c = r == 20 ? 0x3f803f80u : 0u; v0 = (u32x4){c, c, c, c}; v1 = v0; }
    f32x16 s = {};
    s = MFMA16(k.m, q.m, s); s = MFMA8(k.t, q.t, s);
    float tm = -INFINITY;
#pragma unroll
    for (int i = 0; i < 16; ++i) {
      const int dlt = 32 * (qt - t) + r - ((i & 3) + 8 * (i >> 2) + 4 * hh);
      if (br && (dlt < 0 || dlt >= 256)) s[i] = -INFINITY;
      tm = fmaxf(tm, s[i]);
    }
    const f32x2 h = halves(tm);
    const float mn = fmaxf(m, fmaxf(h[0], h[1])), al = __builtin_amdgcn_exp2f(m - mn); m = mn;
#pragma unroll
    for (int i = 0; i < 16; ++i) { o[i] *= al; s[i] = __builtin_amdgcn_exp2f(s[i] - mn); }
    o = pv(v0, __builtin_shufflevector(s, s, 0, 1, 2, 3, 4, 5, 6, 7), o);
    o = pv(v1, __builtin_shufflevector(s, s, 8, 9, 10, 11, 12, 13, 14, 15), o);
  }
  return o;
}

struct OPart { int cb, rt0, n; };
__device__ __forceinline__ OPart opart(int k) {
  OPart p; p.n = 6 - ((k + 8) >> 4);
  p.cb = (int)(((k & 16) ? 0x9999888877665544ull : 0x3322110076543210ull) >> (4 * (k & 15))) & 15; p.rt0 = (int)(((k & 16) ? 0xc840c840b6b6b6b6ull : 0xb6b6b6b600000000ull) >> (4 * (k & 15))) & 15; return p; }

__device__ __forceinline__ unsigned head_flag(int br, int head, int seg, int rowu, int hi) { return gflag(br, rowu, 10 * seg + ((20 * head + (hi ? 19 : 0)) >> 6)); }
__device__ __forceinline__ u32x4 stage_offs(int lane, int lw) {
  u32x4 o; o[0] = lane * 16;
#pragma unroll
  for (int i = 0; i < 3; ++i) { const int idx = 64 * (4 * i + lw) + lane, run = (idx * 3277) >> 16; o[1 + i] = run * 10240 + (idx - 20 * run) * 16; }
  return o; }
__device__ __forceinline__ void v_block(u32x4 srd, u32x4 so, unsigned src, unsigned dst, int lw) {
#pragma unroll
  for (int i = 0; i < 3; ++i) if (i < 2 || lw < 2) dma16c(srd, so[1 + i], src, dst + (4 * i + lw) * 1024);
}
__device__ __forceinline__ void kv_block(u32x4 srd, u32x4 so, int head, int n, unsigned dst, int lw) {
#pragma unroll
  for (int i = 0; i < 3; ++i) dma16c(srd, so[0], WS_K + ccol3(head, i) * 32768 + n * 4096 + lw * 1024, dst + i * 4096 + lw * 1024);
  v_block(srd, so, WS_V + n * 327680 + head * 320, dst + 12288, lw);
}
template <int KEEP>
__device__ __forceinline__ void a_sync(u32x4 srd, u32x4 so, int head, int n, int lw ) {
  asm volatile("s_waitcnt vmcnt(%0)\n\ts_barrier" :: "i"(KEEP) : "memory");
  if (n < 6) kv_block(srd, so, head, n + 2, ((n + 2) % 3) * SLOT, lw);
}

__global__ __launch_bounds__(768, 3) void k_fused(const void* xa, const void* wa, const void* woa, const void* xb, const void* wb, const void* wob, char* ws, char* out) {
  const int w = __builtin_amdgcn_readfirstlane(threadIdx.x >> 6);
  const int rowblk = (blockIdx.x & 3) * 4 + ((blockIdx.x >> 3) & 3), colblk = (blockIdx.x & 4) * 2 + (blockIdx.x >> 5);
  const bool has_g = colblk < 15;
  u64 tag;
  if (has_g && w < 4) {
    const int lane = fresh_lane(), xr = lane >> 3;
    const unsigned xv = (rowblk * 128 + 32 * w + xr) * 1280 + (((lane & 7) ^ (xr >> 1)) << 4);
    const unsigned wv = (16 * w + (lane >> 4)) * 3840 + colblk * 256 + ((((lane >> 2) & 3) ^ (lane >> 4)) << 6) + ((lane & 3) << 4);
    const u32x4 xo = {xv, (xv ^ 64) + 8 * 1280 - 1024, xv + 16 * 1280 - 2048, (xv ^ 64) + 24 * 1280 - 3072}, wo = {wv, wv + 4 * 3840 - 1024, (wv ^ 32) + 8 * 3840 - 2048, (wv ^ 32) + 12 * 3840 - 3072};
    const int grp = rowblk + colblk;
    const u64 o01 = (grp & 1) ? 0x9865742130ull : 0x9875463120ull, o23 = (grp & 1) ? 0x8796435201ull : 0x9768532401ull;
    u32x4 sx = make_srd(xa, SEQ * DM * 2), sw = make_srd(wa, DM * 3 * DM * 2); unsigned d = w * 4096;
    asm volatile(LOAD_ASM : "={v[8:9]}"(tag), "+{s[40:43]}"(sx), "+{s[44:47]}"(sw), "+{s54}"(d)
                 : "{v[10:13]}"(xo), "{v[14:17]}"(wo), "{s[48:49]}"((u64)xb & 0xffffffffffffull), "{s[50:51]}"((u64)wb & 0xffffffffffffull),
                   "{s[52:53]}"((grp & 2) ? o23 : o01), "{s[66:67]}"(ws + WS_EPOCH)
                 : "memory", "scc", "s55", "s56", "s57", "s58", "s59", "v18" );
  }
  u32x4 so8, sc8;
  if (later(w) >= 8) {
    const int b8 = later(blockIdx.x), lane = fresh_lane(), jj = b8 >> 3, lw = w - 8, hd = (b8 & 7) * 4 + (jj >> 3), q8 = jj & 7;
    if (has_g) BAR();
    so8 = stage_offs(lane, lw);
    asm volatile("" : "+{v[160:163]}"(so8));
    if (has_g) BAR();
    sc8[0] = (8 * lw + (lane >> 3)) * 1280 + (((lane & 7) ^ (((lane >> 4) & 1) << 2) ^ ((lw & 1) << 1)) << 4); sc8[1] = (64 * lw + lane) * 1280;
    const int ru = 4 * q8 - 4 + ((lane >> 1) & 7);
    sc8[2] = head_flag(1, hd, (lane & 32) ? 0 : 1 + ((lane >> 4) & 1), (lane & 32) ? 4 * q8 + ((lane >> 1) & 3) : (ru < 0 ? 0 : ru), lane & 1);
    sc8[3] = octr((lane >> 3) & 1, 8 * q8 + (lane & 7), hd);
    asm volatile("" : "+{v[0:3]}"(sc8));
    if (has_g) BAR();
  }
  if (has_g && later(w) >= 4) {
    const int lane = fresh_lane(), g = lane >> 4, r = lane & 15, q = (lane >> 2) & 3, ta = w & 1, cb = (w >> 1) & 1, br = w >> 3;
    unsigned slot = br * 65536;
    const unsigned xa0 = slot + (64 * ta + r) * 128 + ((g ^ (r >> 1)) << 4);
    const unsigned wa0 = slot + 16384 + (8 * g + q) * 256 + (((4 * cb) ^ (2 * q) ^ (g & 1)) << 5) + (lane & 3) * 8;
    u32x2 xad = {xa0, xa0 ^ 64}; u32x4 wad = {wa0, wa0 ^ 32, wa0 ^ 64, wa0 ^ 96};
    const bool vt = colblk >= 10;
    const unsigned hs = vt ? 10240 : 32768;
    const unsigned d0 = br * WS_BR + (vt ? WS_V + (16 * rowblk + 8 * ta) * 10240 + (128 * (colblk - 10) + 64 * cb) * 16 : (colblk * 16 + cb * 8) * 32768 + (128 * rowblk + 64 * ta) * 16)
                        + (((g & 1) << 1) | (g >> 1)) * hs + r * 16;
    unsigned pf = (((blockIdx.x >> 3) % 28) * 4 + (w & 3)) * PFS + 1024;
    asm volatile(GEMM_ASM : "={v[8:9]}"(tag), "+{v[20:21]}"(xad), "+{v[22:25]}"(wad), "+{v28}"(pf), "+{s60}"(slot), "+{v[160:163]}"(so8), "+{v[0:3]}"(sc8)
                 : "{v26}"(d0), "{s[40:43]}"(make_srd(ws, WS_BYTES)), "{s44}"(4 * hs), "{s45}"(colblk < 5 ? __float_as_uint(QSCALE) : 0u), "{s64}"(br), "{s65}"((colblk + 6) & 16), "{s[66:67]}"(ws + WS_EPOCH)
                 : "memory", "scc", "s61", "s62", "s63", "v18", "v32", "v33", "v34", "v35", "v36", "v37", "v38", "v39", V10(4), V10(5), V10(6), V10(7), V10(8), V10(9),
                   V10(10), V10(11), V10(12), V10(13), V10(14), V10(15));
  }
  if (__builtin_expect(!has_g, 0)) { tag = ld_flag(ws, WS_EPOCH); asm volatile("" : "+v"(tag)); __builtin_amdgcn_s_sleep(127); __builtin_amdgcn_s_sleep(IDLE_SLEEP); }
  tag += 1;
  const int bid = later(blockIdx.x), xcd = bid & 7, j = bid >> 3, head = xcd * 4 + (j >> 3), qb = j & 7;
  const OPart pa = opart(j), pb_ = opart(31 - j);
  const int one = (0xf >> w) & 1, e5 = (8 - ((w & 1) ? pb_.n : pa.n)) >> 2 & one, obr = ((0xfa >> w) & 1) ^ e5, ov = one * 8 + obr * 4 + (w & 2) + (((0xaa0 >> w) & 1) | e5);
  const u32x4 srd = make_srd(ws, WS_BYTES);
  const int lane = fresh_lane(), qt = 8 * qb + 2 * (w & 3) + ((w >> 2) & 1);
  u32x6 tc; u32x16 wb0, wb1; u32x12 wb2;
  unsigned t12;
  u64 tgt;
  u32x12 tp;
  auto tail_scalars = [&]() {
    const int r0 = 16 * xcd + (obr ? pb_.rt0 : pa.rt0), ra = r0 + 4 * one + (ov & 1) * (2 - one), rz = ra + 1 - one + ((0xa00 >> w) & 1) * (pa.n - 4);
    const u32x4 so_ = make_srd(out, SEQ * 2 * DM * 2);
    tp = (u32x12){so_[0], so_[1], so_[2], so_[3], (2u << ((rz >> 1) - (ra >> 1))) - 1, 0u, (unsigned)r0 * 256, octr(1, qt, head), (unsigned)(2 - 2 * obr + one), (unsigned)(ra >> 1) * 256,
                  w < 2 * pa.n ? (16 * xcd + pa.rt0) * 40960u + pa.cb * 128 : OOB, w < 2 * pb_.n ? (16 * xcd + pb_.rt0) * 40960u + 1280 + pb_.cb * 128 : OOB};
    asm volatile("" : "+{s[84:95]}"(tp));
  };
  if (w < 8) {
    const int hh = lane >> 5, r = lane & 31, kh = w >> 2, wq = qt & 7;
    u32x7 lc;
    lc[0] = hh * 4096 + r * 16; lc[1] = r * 16 + (head & 1) * 8; lc[2] = hh * 320 + vpos(head, r < 20 ? r : 0) * 16; lc[3] = hh * 8192 + r * 16; lc[4] = lane * 16;
    lc[5] = (ccol3(head, 0) + hh) * 32768 + r * 16; lc[6] = hh ? OOB : ccol3(head, 2) * 32768 + lc[1];
    u32x4 p44 = {(unsigned)(qb ? 9 : wq + 1), (unsigned)kh * 2048, 12288u + kh * 5120, (unsigned)L_QA + wq * 512};
    u32x4 p48 = {(unsigned)wq * 512, (unsigned)L_B + 24576 + wq * 1280, (unsigned)L_MRG + w * 3072, (unsigned)L_MRG + (w ^ 4) * 3072};
    u32x3 p52 = {4096u + ov * 1024 + ((20480u + w * 512) << 16), octr(0, qt, head), WS_O + qt * 512};
    unsigned s56 = WS_BR + WS_O + qt * 512, bad;
    asm volatile("" : "+{v[0:6]}"(lc), "+{s[44:47]}"(p44), "+{s[48:51]}"(p48), "+{s[52:54]}"(p52), "+{s56}"(s56));
    if (w < 4 && has_g) asm volatile("s_waitcnt vmcnt(8)\n\ts_barrier" ::: "memory");
    tail_scalars();
    if (w < 4) { if (has_g) asm volatile("s_waitcnt vmcnt(0)\n\ts_barrier" ::: "memory");  }
    else {
      unsigned f0 = head_flag(0, head, 1, lane >> 1, lane & 1), f1 = head_flag(0, head, 2, lane >> 1, lane & 1), f2 = head_flag(0, head, 0, 4 * qb + ((lane >> 1) & 3), lane & 1);
      bool ok = false;
      u32x4 so = stage_offs(lane, w - 4);
      asm volatile("" : "+v"(so), "+v"(f0), "+v"(f1), "+v"(f2));
      if (has_g) {
        for (int i = 12; i <= FLAG_AT; ++i) BAR();
        WAITV(0); st_flag(ws, gflag(0, 2 * rowblk + (w & 1), 2 * colblk + ((w >> 1) & 1)), tag, lane);
        for (int i = FLAG_AT + 1; i <= SAMPLE_AT; ++i) BAR();
        const u64 a = ld_flag(ws, f0), b = ld_flag(ws, f1), c = ld_flag(ws, f2);
        for (int i = SAMPLE_AT + 1; i <= CHECK_AT; ++i) BAR();
        ok = __all(a == tag && b == tag && c == tag);
        if (!ok) for (int i = CHECK_AT + 1; i <= 19; ++i) BAR();
      }
      if (!ok) poll3(ws, f0, f1, f2, tag);

#pragma unroll
      for (int i = 0; i < 3; ++i) dma16c(srd, so[0], WS_Q + ccol3(head, i) * 32768 + qb * 4096 + (w - 4) * 1024, L_QA + i * 4096 + (w - 4) * 1024);
      kv_block(srd, so, head, 0, 0, w - 4);
      if (ok && CHECK_AT < 19) BAR();
      kv_block(srd, so, head, 1, SLOT, w - 4);
      __builtin_amdgcn_s_setprio(0);
    }
    tgt = tag * 32 + 16;
    asm volatile(ATTN_ASM
                 : "={s63}"(bad), "+{s[44:47]}"(p44), "+{s[48:51]}"(p48), "+{s[52:54]}"(p52), "+{s56}"(s56), "+{v[0:6]}"(lc), "={v[160:165]}"(tc), "+{v[8:9]}"(tgt), "+{s[84:95]}"(tp)
                 : "{s[40:43]}"(srd), "{s[58:59]}"(&TAB)
                 : "memory", "scc", "vcc", "s55", "s57", "s60", "s61", "s62", "s64", "s65", "s66", "s67", "s68", "s69",
                   V10(1), V10(2), V10(3), V10(4), V10(5), V10(6), V10(7), V10(8), V10(9), V10(10), V10(11), V10(12), V10(13), V10(14), V10(15));
    if (__builtin_expect(bad, 0)) {
      const rsrc_t rw = make_rsrc(ws, WS_BYTES);
#pragma unroll 1
      for (int br = 0; br < 2; ++br) if (bad >> br & 1) {
        const int l = lc[4] >> 4;
        const f32x16 o = attn_exact(rw, br, head, qt, l);
        const float inv = __builtin_amdgcn_rcpf(halves(o[8])[1]);
        const unsigned base = br * WS_BR + WS_O + qt * 512;
        __builtin_amdgcn_raw_buffer_store_b128((u32x4){pk2(o[0] * inv, o[1] * inv), pk2(o[2] * inv, o[3] * inv), pk2(o[4] * inv, o[5] * inv), pk2(o[6] * inv, o[7] * inv)}, rw, base + lc[5], 0, SC1);
        __builtin_amdgcn_raw_buffer_store_b64((u32x2){pk2(o[8] * inv, o[9] * inv), pk2(o[10] * inv, o[11] * inv)}, rw, base + lc[6], 0, SC1);
        if (!br) { WAITV(0); arrive(ws, octr(0, qt, head), l); BAR(); }
      }
    }
  } else {
    __builtin_amdgcn_s_setprio(3);
    const int lw = w - 8;
    const u32x4 so = so8;
    if (!has_g) BAR();
     BAR();  kv_block(srd, so, head, 2, 2 * SLOT, lw);
    const unsigned wv = sc8[0], wl = sc8[1];
    const rsrc_t rb = make_rsrc((const char*)wob + pb_.cb * 128, DM * DM * 2 - pb_.cb * 128);
    const u32x4 sa = make_srd((const char*)woa + pa.cb * 128, DM * DM * 2 - pa.cb * 128), sb = make_srd((const char*)wob + pb_.cb * 128, DM * DM * 2 - pb_.cb * 128);
    auto wbl = [&](int i) -> u32x4 { return __builtin_amdgcn_raw_buffer_load_b128(rb, wv, i * 40960, 0); };
    auto warm = [&](u32x4 sr, int row) {
      asm volatile("s_mov_b32 m0, %3\n\ts_nop 0\n\tbuffer_load_dword %0, %1, %2 offen lds" :: "v"(wl), "s"(sr), "s"(row * 1280), "s"(L_QB + 12288 + lw * 256) : "memory"); };
#define CAT4(a, b, c, d) __builtin_shufflevector(__builtin_shufflevector(a, b, 0, 1, 2, 3, 4, 5, 6, 7), __builtin_shufflevector(c, d, 0, 1, 2, 3, 4, 5, 6, 7), 0, 1, 2, 3, 4, 5, 6, 7, 8, 9, 10, 11, 12, 13, 14, 15)
    auto wdma = [&](u32x4 sr, unsigned dst, int i0, int i1) {
#pragma unroll 1
      for (int i = i0; i < i1; ++i) dma16(sr, wv, i * 40960, dst + (4 * i + lw) * 1024); };
    a_sync<0>(srd, so, head, 1, lw );
    if (w == 8) st_flag(ws, WS_OF + bid * 128, tag * 32, lane);
    if (has_g) st_flag(ws, gflag(1, 2 * rowblk + (w & 1), 2 * colblk + ((w >> 1) & 1)), tag, lane);
    asm volatile("s_mov_b32 m0, %2\n\ts_nop 0\n\tglobal_load_lds_dword %0, %1" :: "v"(lw * 6144 + so[0] * 6), "s"(&TAB), "s"(L_QB + 12288 + lw * 256) : "memory");
    wb0 = CAT4(wbl(0), wbl(1), wbl(2), wbl(3)); wb1 = CAT4(wbl(4), wbl(5), wbl(6), wbl(7));
    a_sync<8>(srd, so, head, 2, lw );
    { const u32x4 t = wbl(10); const u32x16 u = CAT4(wbl(8), wbl(9), t, t); wb2 = __builtin_shufflevector(u, u, 0, 1, 2, 3, 4, 5, 6, 7, 8, 9, 10, 11); }
    { const u32x4 t0 = *(const u32x4*)TAB.tr[ov][lane]; const u32x2 t1 = *(const u32x2*)TAB.tw[w][lane]; tc = (u32x6){t0[0], t0[1], t0[2], t0[3], t1[0], t1[1]}; }
    wdma(sb, L_B, 17, 20); warm(sb, 320);
    a_sync<4>(srd, so, head, 3, lw );
    warm(sa, 0); warm(sa, 256);
    a_sync<2>(srd, so, head, 4, lw );
    {
      const int pb = __builtin_amdgcn_readfirstlane(qb ? qb - 1 : 0);
      poll1(ws, sc8[2], tag);
#pragma unroll
      for (int i = 0; i < 3; ++i) {
        const unsigned c = WS_BR + ccol3(head, i) * 32768 + lw * 1024;
        dma16c(srd, so[0], WS_K + c + pb * 4096, L_B + i * 8192 + lw * 1024); dma16c(srd, so[0], WS_K + c + qb * 4096, L_B + i * 8192 + 4096 + lw * 1024);
        dma16c(srd, so[0], WS_Q + c + qb * 4096, L_QB + i * 4096 + lw * 1024);
      }
      v_block(srd, so, WS_BR + WS_V + pb * 327680 + head * 320, L_B + 24576, lw); v_block(srd, so, WS_BR + WS_V + qb * 327680 + head * 320, L_B + 24576 + 10240, lw);
    }
    a_sync<13>(srd, so, head, 5, lw );
    if (w == 8) {
#pragma unroll 1
      for (int spin = 0; spin < (1 << 20); ++spin) {
        u64 v = tag * 32;
        if (lane < 16) v = __hip_atomic_fetch_add((GLB_AS u64*)(ws + sc8[3]), opaque0(), __ATOMIC_RELAXED, __HIP_MEMORY_SCOPE_AGENT);
        if (__all(v - tag * 32 < 32)) break;
        __builtin_amdgcn_s_sleep(8);
      }
    }
    if (w == 9 && bid == 255) {
#pragma unroll 1
      for (int spin = 0; spin < (1 << 20); ++spin) {
        u64 v[4];
#pragma unroll
        for (int i = 0; i < 4; ++i) v[i] = __hip_atomic_fetch_add((GLB_AS u64*)(ws + WS_OF + (64 * i + lane) * 128), opaque0(), __ATOMIC_RELAXED, __HIP_MEMORY_SCOPE_AGENT) - tag * 32;
        if (__all((v[0] | v[1] | v[2] | v[3]) < 32)) break;
        __builtin_amdgcn_s_sleep(8);
      }
      st_flag(ws, WS_EPOCH, tag, lane);
    }
    if (w == 10) { u64 pc;
      asm volatile("s_getpc_b64 %0\n.Lk_pc:\n\ts_add_u32 s62, s62, .Lk_win-.Lk_pc\n\ts_addc_u32 s63, s63, 0\n\ts_mov_b32 m0, %2\n\ts_nop 0\n\tglobal_load_lds_dword %1, %0"
                   : "={s[62:63]}"(pc) : "v"(so[0] * 8), "s"(L_QB + 12288 + lw * 256) : "memory", "scc"); }
    warm(sa, 512);
    a_sync<1>(srd, so, head, 6, lw );
    wdma(sa, 0, 11, 16);
    a_sync<5>(srd, so, head, 7, lw );
    wdma(sa, 0, 0, 5);
    tail_scalars(); tgt = tag * 32 + 16; t12 = L_B + lw * 1024 + so[0];
    BAR(); BAR();
    {
      __builtin_amdgcn_s_sleep(W_SLEEP);
      wdma(sa, 0, 5, 11); wdma(sa, 0, 16, 20); wdma(sb, L_B, 11, 17);
      __builtin_amdgcn_s_sleep(PRE_SLEEP);

    }
  }

  asm volatile(TAIL_ASM
               : "+{v12}"(t12), "+{s[84:95]}"(tp), "+{v[112:127]}"(wb0), "+{v[128:143]}"(wb1), "+{v[144:155]}"(wb2)
               : "{v[160:165]}"(tc), "{v[8:9]}"(tgt), "{s[40:43]}"(srd)
               : "memory", "scc", "vcc", "s50", "v0", "v1", "v2", "v3", "v4", "v5", "v6", "v7", "v10", "v11", "v13", "v14", "v15", "v16", "v17", "v18", "v19",
                 V10(2), V10(3), V10(4), V10(5), V10(6), V10(7), V10(8), V10(9), V10(10), "v110", "v111", "v156", "v157", "v158", "v159", "v166");
}

extern "C" void kernel_launch(void* const* d_in, const int* in_sizes, int n_in, void* d_out, int out_size, void* d_ws, size_t ws_size, hipStream_t stream) {
  (void)hipFuncSetAttribute((const void*)k_fused, hipFuncAttributeMaxDynamicSharedMemorySize, LDS_BYTES);
  k_fused<<<256, 768, LDS_BYTES, stream>>>(d_in[0], d_in[1], d_in[2], d_in[3], d_in[4], d_in[5], (char*)d_ws, (char*)d_out);
}
```
